```python
import torch

def _softmax(x, axis=-1):
    return torch.softmax(x, dim=axis)
def _gelu(x):
    return torch.nn.functional.gelu(x, approximate="tanh")
def _silu(x):
    return torch.nn.functional.silu(x)
def _sigmoid(x):
    return torch.sigmoid(x)
def _layernorm(x, axis=-1, eps=1e-5):
    m = x.mean(dim=axis, keepdim=True)
    v = x.var(dim=axis, keepdim=True, unbiased=False)
    return (x - m) / torch.sqrt(v + eps)
def _scatter_add(tgt, idx, src, axis=0):
    return tgt.clone().index_add_(axis, idx, src)
def _segment_sum(src, seg, n):
    out = torch.zeros((n,) + src.shape[1:], dtype=src.dtype)
    return out.index_add_(0, seg, src)
def _pad(x, pw):
    flat = tuple(v for pair in reversed(pw) for v in pair)
    return torch.nn.functional.pad(x, flat)

kernel_name = 'interleave_layers__spectral_fnet__glu_gated_glu__prompt'

def setup_inputs():

    _w = (lambda a: (lambda t: t.to(torch.bfloat16) if t.is_floating_point() else t)(torch.as_tensor(a)))
    def _A_setup():
        B, S, D = 1, 2048, 576
        ns = torch.arange(S)[:, None] * torch.arange(S)[None, :]
        nd = torch.arange(D)[:, None] * torch.arange(D)[None, :]
        Ws_re = torch.cos(-2.0 * torch.pi * ns / S).to(float)
        Ws_im = torch.sin(-2.0 * torch.pi * ns / S).to(float)
        Wd_re = torch.cos(-2.0 * torch.pi * nd / D).to(float)
        Wd_im = torch.sin(-2.0 * torch.pi * nd / D).to(float)
        return {
            "x": _w((lambda *s: torch.randn(*s, dtype=torch.bfloat16) * 0.05)(B, S, D)),
            "dft_seq_re": _w(Ws_re),
            "dft_seq_im": _w(Ws_im),
            "dft_hid_re": _w(Wd_re),
            "dft_hid_im": _w(Wd_im),
        }
    _A_in = _A_setup()

    def _B_setup():
        B, S, D, F = 1, 2048, 576, 4608
        return {
            "x": _w((lambda *s: torch.randn(*s, dtype=torch.bfloat16) * 0.05)(B, S, D)),
            "w_gate": _w((lambda *s: torch.randn(*s, dtype=torch.bfloat16) * 1.6)(D, F)),
            "w_up": _w((lambda *s: torch.randn(*s, dtype=torch.bfloat16) * 0.4)(D, F)),
            "w_down": _w((lambda *s: torch.randn(*s, dtype=torch.bfloat16) * 0.4)(F, D)),
        }
    _B_in = _B_setup()

    return {
        "A_x": _A_in["x"],
        "A_dft_seq_re": _A_in["dft_seq_re"],
        "A_dft_seq_im": _A_in["dft_seq_im"],
        "A_dft_hid_re": _A_in["dft_hid_re"],
        "A_dft_hid_im": _A_in["dft_hid_im"],
        "B_x": _B_in["x"],
        "B_w_gate": _B_in["w_gate"],
        "B_w_up": _B_in["w_up"],
        "B_w_down": _B_in["w_down"],
    }

def reference(A_x, A_dft_seq_re, A_dft_seq_im, A_dft_hid_re, A_dft_hid_im, B_x, B_w_gate, B_w_up, B_w_down):
    def _A_ref(x, dft_seq_re, dft_seq_im, dft_hid_re, dft_hid_im):
        a = torch.einsum("bsd,ed->bse", x, dft_hid_re)
        b = torch.einsum("bsd,ed->bse", x, dft_hid_im)
        y = torch.einsum("ts,bse->bte", dft_seq_re, a) - torch.einsum("ts,bse->bte", dft_seq_im, b)
        return y

    def _B_ref(x, w_gate, w_up, w_down):
        g = torch.einsum("bsd,df->bsf", x, w_gate)
        u = torch.einsum("bsd,df->bsf", x, w_up)
        h = _sigmoid(g) * u
        return torch.einsum("bsf,fd->bsd", h, w_down)

    x = A_x
    x = x + _A_ref(_layernorm(x, axis=-1), A_dft_seq_re, A_dft_seq_im, A_dft_hid_re, A_dft_hid_im)
    x = x + _B_ref(_layernorm(x, axis=-1), B_w_gate, B_w_up, B_w_down)
    x = x + _A_ref(_layernorm(x, axis=-1), A_dft_seq_re, A_dft_seq_im, A_dft_hid_re, A_dft_hid_im)
    x = x + _B_ref(_layernorm(x, axis=-1), B_w_gate, B_w_up, B_w_down)
    return x
```

```cpp
#include <hip/hip_runtime.h>
#include <hip/hip_bf16.h>
typedef __hip_bfloat16 bf16;
constexpr int S = 2048, D = 576, F = 4608;

__global__ void k_ln(const bf16* x, bf16* xn) {
  int row = blockIdx.x;
  int t = threadIdx.x;
  float v[9];
  float s = 0.f;
  for (int i = 0; i < 9; ++i) { v[i] = __bfloat162float(x[row * D + t + 64 * i]); s += v[i]; }
  for (int o = 32; o; o >>= 1) s += __shfl_xor(s, o);
  float m = s / D;
  float q = 0.f;
  for (int i = 0; i < 9; ++i) { float d = v[i] - m; q += d * d; }
  for (int o = 32; o; o >>= 1) q += __shfl_xor(q, o);
  float r = rsqrtf(q / D + 1e-5f);
  for (int i = 0; i < 9; ++i) xn[row * D + t + 64 * i] = __float2bfloat16((v[i] - m) * r);
}

template <bool BT>
__global__ void k_gemm(const bf16* A, const bf16* B, float* C, int M, int N, int K) {
  __shared__ float As[16][65];
  __shared__ float Bs[16][65];
  int bm = blockIdx.y * 64, bn = blockIdx.x * 64;
  int tx = threadIdx.x & 15, ty = threadIdx.x >> 4;
  float acc[4][4] = {};
  for (int k0 = 0; k0 < K; k0 += 16) {
    for (int i = threadIdx.x; i < 1024; i += 256) {
      int r = i >> 4, c = i & 15;
      As[c][r] = __bfloat162float(A[(size_t)(bm + r) * K + k0 + c]);
      if (BT) Bs[c][r] = __bfloat162float(B[(size_t)(bn + r) * K + k0 + c]);
      else { int kk = i >> 6, nn = i & 63; Bs[kk][nn] = __bfloat162float(B[(size_t)(k0 + kk) * N + bn + nn]); }
    }
    __syncthreads();
    for (int k = 0; k < 16; ++k) {
      float a[4], b[4];
      for (int i = 0; i < 4; ++i) { a[i] = As[k][ty * 4 + i]; b[i] = Bs[k][tx * 4 + i]; }
      for (int i = 0; i < 4; ++i) for (int j = 0; j < 4; ++j) acc[i][j] += a[i] * b[j];
    }
    __syncthreads();
  }
  for (int i = 0; i < 4; ++i) for (int j = 0; j < 4; ++j) C[(size_t)(bm + ty * 4 + i) * N + bn + tx * 4 + j] = acc[i][j];
}
__global__ void k_cvt(const float* a, bf16* o, int n) { int i = blockIdx.x * 256 + threadIdx.x; if (i < n) o[i] = __float2bfloat16(a[i]); }
__global__ void k_res2(const bf16* x, const float* p, const float* q, bf16* o, int n) {
  int i = blockIdx.x * 256 + threadIdx.x; if (i < n) o[i] = __float2bfloat16(__bfloat162float(x[i]) + (p[i] - (q ? q[i] : 0.f)));
}
__global__ void k_glu(const float* g, const float* u, bf16* h, int n) {
  int i = blockIdx.x * 256 + threadIdx.x; if (i < n) h[i] = __float2bfloat16(u[i] / (1.f + __expf(-g[i])));
}

extern "C" void kernel_launch(void* const* d_in, const int* in_sizes, int n_in, void* d_out, int out_size, void* d_ws, size_t ws_size, hipStream_t stream) {
  const bf16* x0 = (const bf16*)d_in[0];
  const bf16* Cr = (const bf16*)d_in[1];
  const bf16* Ci = (const bf16*)d_in[2];
  const bf16* Wr = (const bf16*)d_in[3];
  const bf16* Wi = (const bf16*)d_in[4];
  const bf16* Wg = (const bf16*)d_in[6];
  const bf16* Wu = (const bf16*)d_in[7];
  const bf16* Wd = (const bf16*)d_in[8];
  char* ws = (char*)d_ws;
  float* f0 = (float*)ws;
  float* f1 = f0 + (size_t)S * F;
  bf16* xn = (bf16*)(f1 + (size_t)S * F);
  bf16* a = xn + S * D;
  bf16* b = a + S * D;
  bf16* xa = b + S * D;
  bf16* xb = xa + S * D;
  bf16* h = xb + S * D;
  const bf16* x = x0;
  int n = S * D;
  for (int l = 0; l < 2; ++l) {
    k_ln<<<S, 64, 0, stream>>>(x, xn);
    k_gemm<true><<<dim3(D / 64, S / 64), 256, 0, stream>>>(xn, Wr, f0, S, D, D);
    k_gemm<true><<<dim3(D / 64, S / 64), 256, 0, stream>>>(xn, Wi, f1, S, D, D);
    k_cvt<<<(n + 255) / 256, 256, 0, stream>>>(f0, a, n);
    k_cvt<<<(n + 255) / 256, 256, 0, stream>>>(f1, b, n);
    k_gemm<false><<<dim3(D / 64, S / 64), 256, 0, stream>>>(Cr, a, f0, S, D, S);
    k_gemm<false><<<dim3(D / 64, S / 64), 256, 0, stream>>>(Ci, b, f1, S, D, S);
    k_res2<<<(n + 255) / 256, 256, 0, stream>>>(x, f0, f1, xa, n);
    k_ln<<<S, 64, 0, stream>>>(xa, xn);
    k_gemm<false><<<dim3(F / 64, S / 64), 256, 0, stream>>>(xn, Wg, f0, S, F, D);
    k_gemm<false><<<dim3(F / 64, S / 64), 256, 0, stream>>>(xn, Wu, f1, S, F, D);
    k_glu<<<(S * F + 255) / 256, 256, 0, stream>>>(f0, f1, h, S * F);
    k_gemm<false><<<dim3(D / 64, S / 64), 256, 0, stream>>>(h, Wd, f0, S, D, F);
    bf16* o = (l == 1) ? (bf16*)d_out : xb;
    k_res2<<<(n + 255) / 256, 256, 0, stream>>>(xa, f0, nullptr, o, n);
    x = o;
  }
}
```

```cpp
#include <hip/hip_runtime.h>
#include <hip/hip_bf16.h>
#include <stdint.h>
#include <type_traits>

typedef unsigned short u16;
typedef unsigned v4u __attribute__((ext_vector_type(4)));
typedef unsigned v2u __attribute__((ext_vector_type(2)));
typedef short v4s __attribute__((ext_vector_type(4)));
typedef short v8s __attribute__((ext_vector_type(8)));
typedef float f32x2 __attribute__((ext_vector_type(2)));
typedef float f32x4 __attribute__((ext_vector_type(4)));
typedef float f32x16 __attribute__((ext_vector_type(16)));
typedef __bf16 bf16x2 __attribute__((ext_vector_type(2)));
typedef __bf16 bf16x8 __attribute__((ext_vector_type(8)));
#define GAS __attribute__((address_space(1)))
#define LAS __attribute__((address_space(3)))
#define DEVI __device__ __forceinline__

constexpr int S = 2048, D = 576, F = 4608;
constexpr float LN_EPS = 1e-5f;

constexpr size_t SZ_X = (size_t)S * D * 2;
constexpr size_t SZ_AB = 2 * SZ_X;
constexpr size_t SZ_H = (size_t)S * F * 2;
constexpr size_t SZ_WDT = (size_t)F * D * 2;
constexpr size_t SZ_P = 4 * SZ_X;
constexpr size_t OFF_XN = 0;
constexpr size_t OFF_XC = OFF_XN + 4 * SZ_X;
constexpr size_t OFF_AB = OFF_XC + 3 * SZ_X;
constexpr size_t OFF_P = OFF_AB + 2 * SZ_AB;
constexpr size_t OFF_H = OFF_P + 4 * SZ_P;
constexpr size_t OFF_WDT = OFF_H + 2 * SZ_H;
constexpr size_t WS_END = OFF_WDT + SZ_WDT;

DEVI float u2f(unsigned u) { return __builtin_bit_cast(float, u); }
DEVI float bflo(unsigned u) { return u2f(u << 16); }
DEVI float bfhi(unsigned u) { return u2f(u & 0xffff0000u); }
DEVI unsigned pack2(float a, float b) {
  f32x2 v = {a, b};
  return __builtin_bit_cast(unsigned, __builtin_convertvector(v, bf16x2));
}
DEVI f32x4 mfma16(v4u a, v4u b, f32x4 c) {
  return __builtin_amdgcn_mfma_f32_16x16x32_bf16(__builtin_bit_cast(bf16x8, a), __builtin_bit_cast(bf16x8, b), c, 0, 0, 0);
}
DEVI f32x16 mfma32(v4u a, v4u b, f32x16 c) {
  return __builtin_amdgcn_mfma_f32_32x32x16_bf16(__builtin_bit_cast(bf16x8, a), __builtin_bit_cast(bf16x8, b), c, 0, 0, 0);
}
DEVI v4u lds128(unsigned addr) { return *(const LAS v4u*)(uintptr_t)addr; }
DEVI v4u trfrag(unsigned a0, unsigned a1) {
  v4s x = __builtin_amdgcn_ds_read_tr16_b64_v4i16((LAS v4s*)(uintptr_t)a0);
  v4s y = __builtin_amdgcn_ds_read_tr16_b64_v4i16((LAS v4s*)(uintptr_t)a1);
  return __builtin_bit_cast(v4u, __builtin_shufflevector(x, y, 0, 1, 2, 3, 4, 5, 6, 7));
}
DEVI unsigned xa_chunk(unsigned s, unsigned c8) {
  unsigned G = s >> 5, u = s & 31;
  unsigned r = 4 * (u >> 3) + (u & 3), t = (u >> 2) & 1;
  return ((G * 18 + (c8 >> 2)) * 2 + t) * 64 + (c8 & 3) * 16 + r;
}
DEVI unsigned xb_chunk(unsigned s, unsigned c8) {
  return ((s >> 5) * 36 + (c8 >> 1)) * 64 + (c8 & 1) * 32 + (s & 31);
}
DEVI unsigned xc_chunk(unsigned s, unsigned c8) {
  return ((s >> 4) * 72 + c8) * 16 + (s & 15);
}
DEVI void glds16(const void* sbase, unsigned voff, unsigned ldsdst) {
  unsigned keep;
  asm volatile("s_mov_b32 %0, m0\n\ts_mov_b32 m0, %3\n\ts_nop 0\n\tglobal_load_lds_dwordx4 %1, %2\n\ts_mov_b32 m0, %0"
               : "=&s"(keep) : "v"(voff), "s"(sbase), "s"(ldsdst) : "memory");
}
template <int IMM> DEVI void gload4(v4u& dst, unsigned voff, const void* sbase) {
  asm volatile("global_load_dwordx4 %0, %1, %2 offset:%3" : "=v"(dst) : "v"(voff), "s"(sbase), "n"(IMM) : "memory");
}
#define GLOAD4(dst, voff, sbase, imm) gload4<imm>(dst, voff, sbase)
#define ST_AUX 16
typedef __amdgpu_buffer_rsrc_t rsrc_t;
DEVI rsrc_t mk_rsrc(const void* p) { return __builtin_amdgcn_make_buffer_rsrc(const_cast<void*>(p), 0, 0x7fffffff, 0x00020000); }
DEVI void st16(rsrc_t rs, unsigned chunk, v4u v) { __builtin_amdgcn_raw_buffer_store_b128(v, rs, chunk * 16, 0, ST_AUX); }
DEVI void st8(rsrc_t rs, unsigned off8, v2u v) { __builtin_amdgcn_raw_buffer_store_b64(v, rs, off8 * 8, 0, ST_AUX); }
#define SB() __builtin_amdgcn_sched_barrier(0)
template <int N> DEVI void wait_vm() { asm volatile("s_waitcnt vmcnt(%0)" ::"n"(N) : "memory"); }
template <int I, int N, class Fn> DEVI void static_for(Fn&& fn) {
  if constexpr (I < N) { fn(std::integral_constant<int, I>{}); static_for<I + 1, N>(fn); }
}
DEVI unsigned opaque(unsigned a) { asm volatile("" : "+v"(a)); return a; }
DEVI void pin(v4u& a) { asm volatile("" : "+v"(a)); }

#define TL_BEGIN(k)
#define TL_END(k)
#define TLARG
#define TLPASS(k)
#define STAMP(i)
#define DIAG_DECL
#define DIAG_OUT(base, nw)

template <int NP, int XOLD, int OUT>
DEVI void comb_body(unsigned blk, const char* __restrict__ xold, const char* __restrict__ part, char* __restrict__ xnew,
                    char* __restrict__ xn) {
  __shared__ f32x2 red[3][8];
  const int lane = threadIdx.x & 63, wv = threadIdx.x >> 6;
  const unsigned tok = blk * 8 + (lane & 7), seg = threadIdx.x >> 3;
  v4u xo[3], pp[NP > 0 ? NP : 1][3];
#pragma unroll
  for (int i = 0; i < 3; ++i) {
    unsigned c8 = seg + 24 * i;
    xo[i] = ((const v4u*)xold)[XOLD == 0 ? tok * 72 + c8 : xc_chunk(tok, c8)];
#pragma unroll
    for (int k = 0; k < NP; ++k) pp[k][i] = ((const v4u*)(part + k * SZ_X))[xc_chunk(tok, c8)];
  }
  float v[3][8];
  float s = 0.f;
#pragma unroll
  for (int i = 0; i < 3; ++i) {
    if (NP > 0) {
#pragma unroll
      for (int j = 0; j < 4; ++j) {
        float lo = 0.f, hi = 0.f;
#pragma unroll
        for (int k = 0; k < NP; ++k) { lo += bflo(pp[k][i][j]); hi += bfhi(pp[k][i][j]); }
        xo[i][j] = pack2(bflo(xo[i][j]) + lo, bfhi(xo[i][j]) + hi);
      }
      unsigned c8 = seg + 24 * i;
      if (OUT == 0) st16(mk_rsrc(xnew), tok * 72 + c8, xo[i]);
      else if (OUT != 3) st16(mk_rsrc(xnew), xc_chunk(tok, c8), xo[i]);
    }
#pragma unroll
    for (int j = 0; j < 4; ++j) { v[i][2 * j] = bflo(xo[i][j]); v[i][2 * j + 1] = bfhi(xo[i][j]); s += v[i][2 * j] + v[i][2 * j + 1]; }
  }
  if (OUT == 0) return;
  s += __shfl_xor(s, 8); s += __shfl_xor(s, 16); s += __shfl_xor(s, 32);
  const float lm = s * (1.f / 192.f);
  float q = 0.f;
#pragma unroll
  for (int i = 0; i < 3; ++i)
#pragma unroll
    for (int j = 0; j < 8; ++j) { float d = v[i][j] - lm; q += d * d; }
  q += __shfl_xor(q, 8); q += __shfl_xor(q, 16); q += __shfl_xor(q, 32);
  if (lane < 8) red[wv][lane] = f32x2{lm, q};
  __syncthreads();
  f32x2 a = red[0][lane & 7], b = red[1][lane & 7], c = red[2][lane & 7];
  const float mean = (a[0] + b[0] + c[0]) * (1.f / 3.f);
  const float da = a[0] - mean, db = b[0] - mean, dc = c[0] - mean;
  const float m2 = a[1] + b[1] + c[1] + 192.f * (da * da + db * db + dc * dc);
  const float rstd = __builtin_amdgcn_rsqf(m2 * (1.f / D) + LN_EPS);
  const float nb = -mean * rstd;
#pragma unroll
  for (int i = 0; i < 3; ++i) {
    unsigned c8 = seg + 24 * i;
    v4u o = {pack2(v[i][0] * rstd + nb, v[i][1] * rstd + nb), pack2(v[i][2] * rstd + nb, v[i][3] * rstd + nb),
             pack2(v[i][4] * rstd + nb, v[i][5] * rstd + nb), pack2(v[i][6] * rstd + nb, v[i][7] * rstd + nb)};
    st16(mk_rsrc(xn), OUT == 2 ? xb_chunk(tok, c8) : xa_chunk(tok, c8), o);
  }
}
DEVI void wdt_task(unsigned task, const u16* __restrict__ Wd, char* __restrict__ wdt) {
  unsigned f8 = task / 144, p = task % 144;
  const v2u* src = (const v2u*)(Wd + (size_t)(8 * f8) * D + 4 * p);
  v2u v[8];
#pragma unroll
  for (int i = 0; i < 8; ++i) v[i] = src[(size_t)i * (D / 4)];
  unsigned cb4 = p / 36, pl = p % 36;
  unsigned k32 = f8 >> 2, q = f8 & 3;
  unsigned frag = pl < 32 ? (2 * (pl >> 3) + (pl & 1)) : 8;
  unsigned r0 = pl < 32 ? 4 * ((pl >> 1) & 3) : 4 * (pl - 32);
  unsigned dst = ((cb4 * 144 + k32) * 9 + frag) * 64 + 16 * q + r0;
#pragma unroll
  for (int dd = 0; dd < 4; ++dd) {
    v4u o;
#pragma unroll
    for (int i = 0; i < 4; ++i) {
      unsigned lo = v[2 * i][dd >> 1], hi = v[2 * i + 1][dd >> 1];
      o[i] = (dd & 1) ? ((lo >> 16) | (hi & 0xffff0000u)) : ((lo & 0xffffu) | (hi << 16));
    }
    st16(mk_rsrc(wdt), dst + dd, o);
  }
}
constexpr int WDT_BLKS = 216;
template <int NP, int XOLD, int OUT, int WH>
__global__ __launch_bounds__(192) void k_comb(const char* __restrict__ xold, const char* __restrict__ part, char* __restrict__ xnew,
                                              char* __restrict__ xn, const u16* __restrict__ Wd, char* __restrict__ wdt TLARG) {
  TL_BEGIN(kid);
  if (WH >= 0 && blockIdx.x >= 256) {
    wdt_task(WH * (WDT_BLKS * 192) + (blockIdx.x - 256) * 192 + threadIdx.x, Wd, wdt);
    TL_END(kid);
    return;
  }
  comb_body<NP, XOLD, OUT>(blockIdx.x, xold, part, xnew, xn);
  TL_END(kid);
}

constexpr int HID_LDS = 81 * 1024;
__global__ __launch_bounds__(512, 2) void k_hid(const char* __restrict__ xa, const u16* __restrict__ Wr, const u16* __restrict__ Wi,
                                                char* __restrict__ ab TLARG) {
  extern __shared__ __attribute__((aligned(16))) char smem[];
  TL_BEGIN(kid);
  const int lane = threadIdx.x & 63;
  const int w8 = __builtin_amdgcn_readfirstlane(threadIdx.x >> 6);
  const int w = w8 & 3;
  const int g8 = blockIdx.x & 7, idx = blockIdx.x >> 3;
  const int tt = (g8 >> 1) * 4 + (idx >> 3);
  const int cp = (g8 & 1) * 8 + (idx & 7);
  const int part = cp >> 3, cb4 = (cp >> 1) & 3, half = cp & 1;
  const int r = lane & 15, q = lane >> 4;
  const int G = 4 * tt + w;
  const unsigned lds0 = (unsigned)(uintptr_t)(LAS char*)smem;
  if (w8 >= 4) {
    const char* Wb = (const char*)((part ? Wi : Wr) + (size_t)(144 * cb4) * D);
    const unsigned rl = lane >> 3;
#pragma unroll
    for (int g = 0; g < 3; ++g)
#pragma unroll
      for (int i = 0; i < 7; ++i) {
        int n = w + 4 * i; if (n > 26) n = 26;
        int st = n / 9, rb = n - 9 * st;
        unsigned key = rb < 8 ? (((rb & 3) << 1) | ((rl >> 1) & 1)) : (rl >> 1);
        unsigned grow = rb < 8 ? (64 * half + 8 * rb + rl) : (128 + 8 * half + rl);
        unsigned voff = grow * 1152 + (((lane & 7) ^ key) << 4);
        glds16(Wb + (3 * g + st) * 128, voff, lds0 + (27 * g + n) * 1024);
      }
    wait_vm<14>(); __builtin_amdgcn_s_barrier();
    wait_vm<7>();  __builtin_amdgcn_s_barrier();
    wait_vm<0>();  __builtin_amdgcn_s_barrier();
    return;
  }
  const char* xbase = xa + (size_t)G * 36 * 1024;
  const unsigned vx = lane * 16;
  v4u xf[18][2];
  auto issue_x = [&](auto kc) {
    constexpr int ks = decltype(kc)::value;
    const char* xp = xbase + (ks >> 1) * 4096;
    gload4<(ks & 1) * 2048>(xf[ks][0], vx, xp);
    gload4<(ks & 1) * 2048 + 1024>(xf[ks][1], vx, xp);
  };
  f32x4 acc[2][5] = {};
  const unsigned rb0 = lds0 + (8 * (r >> 2) + (r & 3)) * 128 + ((q ^ (r >> 1)) << 4);
  const unsigned rb1 = rb0 ^ 64;
  const unsigned re0 = lds0 + (64 + (r & 7)) * 128 + ((q ^ ((r & 7) >> 1)) << 4);
  const unsigned re1 = re0 ^ 64;
  const unsigned rb0h = opaque(rb0 + 4 * 9216), rb1h = opaque(rb1 + 4 * 9216), re0h = opaque(re0 + 4 * 9216), re1h = opaque(re1 + 4 * 9216);
  auto waddr = [&](int i) -> unsigned {
    int ks = i / 5, jj = i % 5;
    unsigned so = ((ks >> 1) & 3) * 9216 + (ks >= 16 ? 4 * 9216 : 0);
    const bool hi = ks >= 8;
    if (jj < 4) return ((ks & 1) ? (hi ? rb1h : rb1) : (hi ? rb0h : rb0)) + so + (32 * (jj >> 1) + 4 * (jj & 1)) * 128;
    return ((ks & 1) ? (hi ? re1h : re1) : (hi ? re0h : re0)) + so;
  };
  constexpr int LA = 5, R = 6, XD = 9;
  v4u wf[R];
  static_for<0, XD>([&](auto kc) { issue_x(kc); });
  static_for<0, 18>([&](auto kc) {
    constexpr int ks = decltype(kc)::value;
    if constexpr (ks % 6 == 0) {
      __builtin_amdgcn_s_barrier();
      asm volatile("" ::: "memory");
#pragma unroll
      for (int i = 0; i < LA; ++i) wf[(5 * ks + i) % R] = lds128(waddr(5 * ks + i));
    }
    constexpr int younger = (ks + XD <= 18 ? XD - 1 : 17 - ks) * 2;
    wait_vm<younger>();
    pin(xf[ks][0]); pin(xf[ks][1]);
    if constexpr (ks + XD < 18) issue_x(std::integral_constant<int, (ks + XD < 18 ? ks + XD : 0)>{});
#pragma unroll
    for (int jj = 0; jj < 5; ++jj) {
      const int i = 5 * ks + jj;
      if ((i % 30) + LA < 30) wf[(i + LA) % R] = lds128(waddr(i + LA));
      SB();
      acc[0][jj] = mfma16(xf[ks][0], wf[i % R], acc[0][jj]);
      acc[1][jj] = mfma16(xf[ks][1], wf[i % R], acc[1][jj]);
      SB();
    }
  });
  const float sg = part ? -1.f : 1.f;
  const unsigned dst = ((cb4 * 128 + part * 64 + G) * 9) * 64;
  const rsrc_t rs = mk_rsrc(ab);
#pragma unroll
  for (int j = 0; j < 5; ++j) {
    v4u o = {pack2(sg * acc[0][j][0], sg * acc[0][j][1]), pack2(sg * acc[0][j][2], sg * acc[0][j][3]),
             pack2(sg * acc[1][j][0], sg * acc[1][j][1]), pack2(sg * acc[1][j][2], sg * acc[1][j][3])};
    if (j < 4) st16(rs, dst + (4 * half + j) * 64 + lane, o);
    else if (r < 8) st16(rs, dst + 8 * 64 + 16 * q + 8 * half + r, o);
  }
  TL_END(kid);
}

constexpr int MID_ABYTES = 18 * 1024, MID_BUF = 34 * 1024, MID_NB = 4, MID_LDS = MID_NB * MID_BUF;
template <bool SEQ>
__global__ __launch_bounds__(512, 2) void k_mid(const char* __restrict__ b0, const char* __restrict__ b1, const char* __restrict__ at,
                                                char* __restrict__ part TLARG) {
  TL_BEGIN(kid);
  constexpr int NS = SEQ ? 16 : 18, NB = MID_NB, NK32 = SEQ ? 128 : 144;
  extern __shared__ __attribute__((aligned(16))) char smem[];
  const int lane = threadIdx.x & 63;
  const int w8 = __builtin_amdgcn_readfirstlane(threadIdx.x >> 6);
  const int w = w8 & 3;
  const int g8 = blockIdx.x & 7, idx = blockIdx.x >> 3;
  const int kq = g8 >> 1, rt = 8 * (g8 & 1) + (idx >> 2), cb4 = idx & 3;
  const int r = lane & 15, q = lane >> 4;
  const unsigned lds0 = (unsigned)(uintptr_t)(LAS char*)smem;
  const unsigned vl = lane * 16;
  if (w8 >= 4) {
    const char* abase = at + ((size_t)(cb4 * NK32 + kq * 2 * NS) * 9) * 1024 + w * 1024;
    const char* bbase;
    unsigned voffE, voffO;
    if constexpr (SEQ) {
      bbase = ((kq >> 1) ? b1 : b0) + ((size_t)(128 * rt + 32 * w) * S + (kq & 1) * 1024) * 2;
      voffE = (lane >> 3) * 4096 + (((lane & 7) ^ (lane >> 4)) << 4);
      voffO = voffE ^ 64;
    } else {
      bbase = b0 + ((size_t)(8 * rt + 2 * w) * 144 + 36 * kq) * 1024;
      voffE = vl; voffO = vl + 1024;
    }
    auto issue = [&](int t) {
      const unsigned buf = lds0 + (t % NB) * MID_BUF;
      const unsigned bd = buf + MID_ABYTES + w * 4096;
      if constexpr (SEQ) {
        const char* cb_ = bbase + t * 128;
#pragma unroll
        for (int n = 0; n < 4; ++n) glds16(cb_ + n * 32768, (n & 1) ? voffO : voffE, bd + n * 1024);
      } else {
        const char* h0 = bbase + t * 2048;
#pragma unroll
        for (int n = 0; n < 4; ++n) glds16(h0 + (n >> 1) * (144 * 1024), (n & 1) ? voffO : voffE, bd + n * 1024);
      }
      const char* ap = abase + t * 18432;
      const unsigned ld = buf + w * 1024;
#pragma unroll
      for (int i = 0; i < 4; ++i) glds16(ap + i * 4096, vl, ld + i * 4096);
      if (w < 2) glds16(ap + 16384, vl, ld + 16384);
    };
    issue(0); issue(1);
#pragma unroll 1
    for (int t = 0; t < NS; ++t) {
      if (t + 1 < NS) { if (w < 2) wait_vm<9>(); else wait_vm<8>(); } else wait_vm<0>();
      __builtin_amdgcn_s_barrier();
      if (t + 2 < NS) issue(t + 2);
    }
    return;
  }
  DIAG_DECL;
  unsigned rdb0, rdb1;
  if constexpr (SEQ) {
    rdb0 = lds0 + MID_ABYTES + w * 4096 + r * 128 + ((q ^ (r >> 1)) << 4);
    rdb1 = rdb0 ^ 64;
  } else {
    rdb0 = lds0 + MID_ABYTES + w * 4096 + vl;
    rdb1 = rdb0 + 1024;
  }
  const unsigned rdb2 = opaque(rdb0 + 2 * MID_BUF), rdb3 = opaque(rdb1 + 2 * MID_BUF);
  v4u bf[2][2][2];
  f32x4 acc[9][2] = {};
  constexpr int LA = 6, R = 7;
  v4u af[R];
  const unsigned ra0 = lds0 + vl, ra1 = opaque(ra0 + 2 * MID_BUF);
  auto aaddr = [&](int i) -> unsigned {
    int t = i / 18, f = i % 18, b = t % NB;
    return ((b & 2) ? ra1 : ra0) + (b & 1) * MID_BUF + f * 1024;
  };
  auto sync = [&](int t) {
    __builtin_amdgcn_s_barrier();
    asm volatile("" ::: "memory");
    const int b = t % NB;
#pragma unroll
    for (int i = 0; i < 2; ++i) {
      bf[t & 1][i][0] = lds128(((b & 2) ? rdb2 : rdb0) + (b & 1) * MID_BUF + i * 2048);
      bf[t & 1][i][1] = lds128(((b & 2) ? rdb3 : rdb1) + (b & 1) * MID_BUF + i * 2048);
    }
  };
  sync(0);
  STAMP(1);
#pragma unroll
  for (int i = 0; i < LA; ++i) af[i % R] = lds128(aaddr(i));
  static_for<0, NS>([&](auto tc) {
    constexpr int t = decltype(tc)::value;
#pragma unroll
    for (int f = 0; f < 18; ++f) {
      const int i = t * 18 + f;
      if (f == 18 - LA && t + 1 < NS) sync(t + 1);
      if (i + LA < NS * 18) af[(i + LA) % R] = lds128(aaddr(i + LA));
      SB();
      acc[f % 9][0] = mfma16(af[i % R], bf[t & 1][0][f / 9], acc[f % 9][0]);
      acc[f % 9][1] = mfma16(af[i % R], bf[t & 1][1][f / 9], acc[f % 9][1]);
      SB();
    }
  });
  const rsrc_t rs = mk_rsrc(part + (size_t)kq * SZ_X);
#pragma unroll
  for (int i = 0; i < 2; ++i) {
    const unsigned dst = ((8 * rt + 2 * w + i) * 72 + 18 * cb4) * 16;
#pragma unroll
    for (int P = 0; P < 4; ++P) {
      v4u o = {pack2(acc[2 * P][i][0], acc[2 * P][i][1]), pack2(acc[2 * P][i][2], acc[2 * P][i][3]),
               pack2(acc[2 * P + 1][i][0], acc[2 * P + 1][i][1]), pack2(acc[2 * P + 1][i][2], acc[2 * P + 1][i][3])};
      st16(rs, dst + P * 64 + lane, o);
    }
    v2u o2 = {pack2(acc[8][i][0], acc[8][i][1]), pack2(acc[8][i][2], acc[8][i][3])};
    st8(rs, (dst + 256) * 2 + ((q >> 1) * 16 + r) * 2 + (q & 1), o2);
  }
  DIAG_OUT(part + (SEQ ? 100u : 104u) * 1048576u, 4);
  TL_END(kid);
}

constexpr int GLU_UOFF = 18560, GLU_BUF = 37120;
constexpr int GLU_LDS = 4 * GLU_BUF;
template <int DL>
DEVI void glu_body(const char* __restrict__ xb, const u16* __restrict__ Wg, const u16* __restrict__ Wu, char* __restrict__ hb,
                   unsigned lds0, int lane, int w, int G, int cb) {
  DIAG_DECL;
  unsigned dvo[5];
  const char* dsb[5];
  unsigned dld[5];
#pragma unroll
  for (int i = 0; i < 5; ++i) {
    int n = w + 8 * i; if (n >= 36) n = 35;
    int mat = n >= 18, m = n - 18 * mat;
    unsigned sl = 64 * m + lane;
    unsigned kr = (sl * 3641u) >> 16, ch = sl - 18 * kr;
    dvo[i] = kr * (F * 2) + ch * 16;
    dsb[i] = (const char*)((mat ? Wu : Wg) + 144 * cb);
    dld[i] = lds0 + mat * GLU_UOFF + m * 1024;
  }
  const char* xbase = xb + (size_t)G * 36 * 1024;
  const unsigned vx = lane * 16;
  v4u xf[2][4];
  auto issue_x = [&](auto sc) {
    constexpr int s = decltype(sc)::value;
    const char* xp_ = xbase + s * 4096;
    GLOAD4(xf[s & 1][0], vx, xp_, 0);    GLOAD4(xf[s & 1][1], vx, xp_, 1024);
    GLOAD4(xf[s & 1][2], vx, xp_, 2048); GLOAD4(xf[s & 1][3], vx, xp_, 3072);
  };
  auto issue_d1 = [&](int t, int i) {
    if (i < 4 || DL == 0) glds16(dsb[i] + (size_t)t * 64 * F * 2, dvo[i], dld[i] + (t & 3) * GLU_BUF);
  };
  auto issue_d = [&](int t) {
#pragma unroll
    for (int i = 0; i < 5; ++i) issue_d1(t, i);
  };
  constexpr int ND = DL == 0 ? 5 : 4;
  const int isu = (lane >> 4) & 1, hf = lane >> 5, qq = (lane & 15) >> 2, p = lane & 3;
  const unsigned tb0 = lds0 + isu * GLU_UOFF + (8 * hf + qq) * 288 + ((((p & 1) << 1) | (p >> 1)) * 8);
  const unsigned tb1 = opaque(tb0 + 2 * GLU_BUF);
  auto waddr = [&](int i) -> unsigned {
    int s = i / 36, ks = (i % 36) / 9, j = i % 9;
    return ((s & 2) ? tb1 : tb0) + (s & 1) * GLU_BUF + ks * 4608 + j * 32;
  };
  f32x16 acc[9];
#pragma unroll
  for (int j = 0; j < 9; ++j) acc[j] = f32x16{};
  constexpr int LA = 4, R = 5;
  v4u wf[R];
  if constexpr (DL == 0) {
    issue_x(std::integral_constant<int, 0>{});
    issue_d(0);
    wait_vm<0>();
    pin(xf[0][0]); pin(xf[0][1]); pin(xf[0][2]); pin(xf[0][3]);
    __builtin_amdgcn_s_barrier();
    asm volatile("" ::: "memory");
#pragma unroll
    for (int i = 0; i < LA; ++i) wf[i % R] = trfrag(waddr(i), waddr(i) + 1152);
    issue_d(1);
  } else {
    issue_d(0);
    issue_d(1);
    wait_vm<ND>();
    __builtin_amdgcn_s_barrier();
    asm volatile("" ::: "memory");
    issue_x(std::integral_constant<int, 0>{});
    issue_d(2);
    wait_vm<ND>();
    pin(xf[0][0]); pin(xf[0][1]); pin(xf[0][2]); pin(xf[0][3]);
    __builtin_amdgcn_s_barrier();
    asm volatile("" ::: "memory");
#pragma unroll
    for (int i = 0; i < LA; ++i) wf[i % R] = trfrag(waddr(i), waddr(i) + 1152);
  }
  STAMP(1);
  static_for<0, 9>([&](auto sc) {
    constexpr int s = decltype(sc)::value;
    constexpr int t = s + DL;
#pragma unroll
    for (int f = 0; f < 36; ++f) {
      const int i = s * 36 + f;
      if (f == 1 && s + 1 < 9) issue_x(std::integral_constant<int, (s + 1 < 9 ? s + 1 : 0)>{});
      if (t + 2 < 9 && f >= 5 && f <= 21 && (f & 3) == 1) issue_d1(t + 2, (f - 5) >> 2);
      if (f == 36 - LA && s + 1 < 9) {
        if constexpr (t + 2 < 9) wait_vm<ND>(); else wait_vm<0>();
        pin(xf[(s + 1) & 1][0]); pin(xf[(s + 1) & 1][1]); pin(xf[(s + 1) & 1][2]); pin(xf[(s + 1) & 1][3]);
        if constexpr (t + 1 < 9) {
          if constexpr (DL == 1) asm volatile("s_waitcnt lgkmcnt(0)" ::: "memory");
          __builtin_amdgcn_s_barrier();
          asm volatile("" ::: "memory");
        }
      }
      if (i + LA < 324) wf[(i + LA) % R] = trfrag(waddr(i + LA), waddr(i + LA) + 1152);
      SB();
      acc[f % 9] = mfma32(wf[i % R], xf[s & 1][f / 9], acc[f % 9]);
      SB();
    }
    STAMP(2 + s);
  });
  const unsigned tok = 32 * G + (lane & 31);
  const unsigned hdst = (tok >> 4) * 144 * 64 + (tok & 15) + hf * 16;
  const rsrc_t rs = mk_rsrc(hb);
#pragma unroll
  for (int j = 0; j < 9; ++j) {
    unsigned ow[4];
#pragma unroll
    for (int i = 0; i < 4; ++i) {
      f32x2 g = {acc[j][2 * i], acc[j][2 * i + 1]};
      f32x2 u = {acc[j][8 + 2 * i], acc[j][9 + 2 * i]};
      g = g * f32x2{-1.44269504088896340736f, -1.44269504088896340736f};
      f32x2 e = {__builtin_amdgcn_exp2f(g[0]), __builtin_amdgcn_exp2f(g[1])};
      e = e + f32x2{1.f, 1.f};
      f32x2 rc = {__builtin_amdgcn_rcpf(e[0]), __builtin_amdgcn_rcpf(e[1])};
      u = u * rc;
      ow[i] = pack2(u[0], u[1]);
    }
    v4u o = {ow[0], ow[1], ow[2], ow[3]};
    unsigned J = 9 * cb + j;
    st16(rs, hdst + (J >> 1) * 64 + (J & 1) * 32, o);
  }
  DIAG_OUT(hb + 70u * 1048576u, 8);
}
__global__ __launch_bounds__(512, 2) void k_glu(const char* __restrict__ xb, const u16* __restrict__ Wg, const u16* __restrict__ Wu,
                                                char* __restrict__ hb TLARG) {
  TL_BEGIN(kid);
  extern __shared__ __attribute__((aligned(16))) char smem[];
  const int lane = threadIdx.x & 63;
  const int w = __builtin_amdgcn_readfirstlane(threadIdx.x >> 6);
  const int g8 = blockIdx.x & 7, idx = blockIdx.x >> 3;
  const int panel = (g8 & 1) * 4 + (idx >> 3), cb = (g8 >> 1) * 8 + (idx & 7);
  const int G = panel * 8 + w;
  const unsigned lds0 = (unsigned)(uintptr_t)(LAS char*)smem;
  if (w < 4) glu_body<0>(xb, Wg, Wu, hb, lds0, lane, w, G, cb);
  else glu_body<1>(xb, Wg, Wu, hb, lds0, lane, w, G, cb);
  TL_END(kid);
}

extern "C" void kernel_launch(void* const* d_in, const int* in_sizes, int n_in, void* d_out, int out_size, void* d_ws,
                              size_t ws_size, hipStream_t stream) {
  const u16* x0 = (const u16*)d_in[0];
  const char* Cr = (const char*)d_in[1];
  const char* Ci = (const char*)d_in[2];
  const u16* Wr = (const u16*)d_in[3];
  const u16* Wi = (const u16*)d_in[4];
  const u16* Wg = (const u16*)d_in[6];
  const u16* Wu = (const u16*)d_in[7];
  const u16* Wd = (const u16*)d_in[8];
  char* ws = (char*)d_ws;
  (void)hipFuncSetAttribute((const void*)k_hid, hipFuncAttributeMaxDynamicSharedMemorySize, HID_LDS);
  (void)hipFuncSetAttribute((const void*)k_mid<true>, hipFuncAttributeMaxDynamicSharedMemorySize, MID_LDS);
  (void)hipFuncSetAttribute((const void*)k_mid<false>, hipFuncAttributeMaxDynamicSharedMemorySize, MID_LDS);
  (void)hipFuncSetAttribute((const void*)k_glu, hipFuncAttributeMaxDynamicSharedMemorySize, GLU_LDS);
  char* xn[4]; char* P[4]; char* xc[3];
  for (int i = 0; i < 4; ++i) { xn[i] = ws + OFF_XN + i * SZ_X; P[i] = ws + OFF_P + i * SZ_P; }
  for (int i = 0; i < 3; ++i) xc[i] = ws + OFF_XC + i * SZ_X;
  char* wdt = ws + OFF_WDT;
  k_comb<0, 0, 3, 0><<<256 + WDT_BLKS, 192, 0, stream>>>((const char*)x0, nullptr, nullptr, xn[0], Wd, wdt TLPASS(0));
  for (int l = 0; l < 2; ++l) {
    char* ab = ws + OFF_AB + l * SZ_AB;
    char* hb = ws + OFF_H + l * SZ_H;
    k_hid<<<256, 512, HID_LDS, stream>>>(xn[2 * l], Wr, Wi, ab TLPASS(1 + 6 * l));
    k_mid<true><<<256, 512, MID_LDS, stream>>>(Cr, Ci, ab, P[2 * l] TLPASS(2 + 6 * l));
    if (l == 0) k_comb<4, 0, 2, 1><<<256 + WDT_BLKS, 192, 0, stream>>>((const char*)x0, P[0], xc[0], xn[1], Wd, wdt TLPASS(3));
    else k_comb<4, 1, 2, -1><<<256, 192, 0, stream>>>(xc[1], P[2], xc[2], xn[3], nullptr, nullptr TLPASS(9));
    k_glu<<<256, 512, GLU_LDS, stream>>>(xn[2 * l + 1], Wg, Wu, hb TLPASS(4 + 6 * l));
    k_mid<false><<<256, 512, MID_LDS, stream>>>(hb, nullptr, wdt, P[2 * l + 1] TLPASS(5 + 6 * l));
    if (l == 0) k_comb<4, 1, 1, -1><<<256, 192, 0, stream>>>(xc[0], P[1], xc[1], xn[2], nullptr, nullptr TLPASS(6));
    else k_comb<4, 1, 0, -1><<<256, 192, 0, stream>>>(xc[2], P[3], (char*)d_out, nullptr, nullptr, nullptr TLPASS(12));
  }
}
```

```cpp
#include <hip/hip_runtime.h>
#include <hip/hip_bf16.h>
#include <stdint.h>
#include <type_traits>

typedef unsigned short u16;
typedef unsigned long long u64;
typedef unsigned v4u __attribute__((ext_vector_type(4)));
typedef unsigned v2u __attribute__((ext_vector_type(2)));
typedef short v4s __attribute__((ext_vector_type(4)));
typedef float f32x2 __attribute__((ext_vector_type(2)));
typedef float f32x4 __attribute__((ext_vector_type(4)));
typedef float f32x16 __attribute__((ext_vector_type(16)));
typedef __bf16 bf16x2 __attribute__((ext_vector_type(2)));
typedef __bf16 bf16x8 __attribute__((ext_vector_type(8)));
#define GAS __attribute__((address_space(1)))
#define LAS __attribute__((address_space(3)))
#define DEVI __device__ __forceinline__
typedef GAS u64 gu64;

constexpr int S = 2048, D = 576, F = 4608;
constexpr float LN_EPS = 1e-5f;

constexpr size_t SZ_X = (size_t)S * D * 2;
constexpr size_t SZ_AB = 2 * SZ_X;
constexpr size_t SZ_H = (size_t)S * F * 2;
constexpr size_t SZ_WDT = (size_t)F * D * 2;
constexpr size_t SZ_P = 4 * SZ_X;
constexpr size_t OFF_FLAG = 0;
constexpr size_t OFF_XN = 65536;
constexpr size_t OFF_AB = OFF_XN + 4 * SZ_X;
constexpr size_t OFF_P = OFF_AB + 2 * SZ_AB;
constexpr size_t OFF_H = OFF_P + 4 * SZ_P;
constexpr size_t OFF_WDT = OFF_H + 2 * SZ_H;
constexpr size_t WS_END = OFF_WDT + SZ_WDT;
constexpr int FG_A = 1, FG_B = FG_A + 16, FG_C = FG_B + 16, FG_D = FG_C + 16, FG_E = FG_D + 8, FG_F = FG_E + 32, FG_LAYER = 104;

DEVI float u2f(unsigned u) { return __builtin_bit_cast(float, u); }
DEVI float bflo(unsigned u) { return u2f(u << 16); }
DEVI float bfhi(unsigned u) { return u2f(u & 0xffff0000u); }
DEVI unsigned pack2(float a, float b) {
  f32x2 v = {a, b};
  return __builtin_bit_cast(unsigned, __builtin_convertvector(v, bf16x2));
}
DEVI f32x4 mfma16(v4u a, v4u b, f32x4 c) {
  return __builtin_amdgcn_mfma_f32_16x16x32_bf16(__builtin_bit_cast(bf16x8, a), __builtin_bit_cast(bf16x8, b), c, 0, 0, 0);
}
DEVI f32x16 mfma32(v4u a, v4u b, f32x16 c) {
  return __builtin_amdgcn_mfma_f32_32x32x16_bf16(__builtin_bit_cast(bf16x8, a), __builtin_bit_cast(bf16x8, b), c, 0, 0, 0);
}
DEVI v4u lds128(unsigned addr) { return *(const LAS v4u*)(uintptr_t)addr; }
DEVI v4u trfrag(unsigned a0, unsigned a1) {
  v4s x = __builtin_amdgcn_ds_read_tr16_b64_v4i16((LAS v4s*)(uintptr_t)a0);
  v4s y = __builtin_amdgcn_ds_read_tr16_b64_v4i16((LAS v4s*)(uintptr_t)a1);
  return __builtin_bit_cast(v4u, __builtin_shufflevector(x, y, 0, 1, 2, 3, 4, 5, 6, 7));
}
DEVI unsigned xa_chunk(unsigned s, unsigned c8) {
  unsigned G = s >> 5, u = s & 31;
  unsigned r = 4 * (u >> 3) + (u & 3), t = (u >> 2) & 1;
  return ((G * 18 + (c8 >> 2)) * 2 + t) * 64 + (c8 & 3) * 16 + r;
}
DEVI unsigned xb_chunk(unsigned s, unsigned c8) {
  return ((s >> 5) * 36 + (c8 >> 1)) * 64 + (c8 & 1) * 32 + (s & 31);
}
DEVI unsigned xc_chunk(unsigned s, unsigned c8) {
  return ((s >> 4) * 72 + c8) * 16 + (s & 15);
}
template <bool SC1> DEVI void glds16(const void* sbase, unsigned voff, unsigned ldsdst) {
  unsigned keep;
  if constexpr (SC1)
    asm volatile("s_mov_b32 %0, m0\n\ts_mov_b32 m0, %3\n\ts_nop 0\n\tglobal_load_lds_dwordx4 %1, %2 sc1\n\ts_mov_b32 m0, %0"
                 : "=&s"(keep) : "v"(voff), "s"(sbase), "s"(ldsdst) : "memory");
  else
    asm volatile("s_mov_b32 %0, m0\n\ts_mov_b32 m0, %3\n\ts_nop 0\n\tglobal_load_lds_dwordx4 %1, %2\n\ts_mov_b32 m0, %0"
                 : "=&s"(keep) : "v"(voff), "s"(sbase), "s"(ldsdst) : "memory");
}
template <int IMM> DEVI void gload4(v4u& dst, unsigned voff, const void* sbase) {
  asm volatile("global_load_dwordx4 %0, %1, %2 offset:%3 sc1" : "=v"(dst) : "v"(voff), "s"(sbase), "n"(IMM) : "memory");
}
typedef __amdgpu_buffer_rsrc_t rsrc_t;
DEVI rsrc_t mk_rsrc(const void* p) { return __builtin_amdgcn_make_buffer_rsrc(const_cast<void*>(p), 0, 0x7fffffff, 0x00020000); }
DEVI void st16(rsrc_t rs, unsigned chunk, v4u v) { __builtin_amdgcn_raw_buffer_store_b128(v, rs, chunk * 16, 0, 16); }
DEVI void st8(rsrc_t rs, unsigned off8, v2u v) { __builtin_amdgcn_raw_buffer_store_b64(v, rs, off8 * 8, 0, 16); }
DEVI v4u ld16(rsrc_t rs, unsigned chunk) { return __builtin_amdgcn_raw_buffer_load_b128(rs, chunk * 16, 0, 16); }
#define SB() __builtin_amdgcn_sched_barrier(0)
template <int N> DEVI void wait_vm() { asm volatile("s_waitcnt vmcnt(%0)" ::"n"(N) : "memory"); }
template <int I, int N, class Fn> DEVI void static_for(Fn&& fn) {
  if constexpr (I < N) { fn(std::integral_constant<int, I>{}); static_for<I + 1, N>(fn); }
}
DEVI unsigned opaque(unsigned a) { asm volatile("" : "+v"(a)); return a; }
template <class T> DEVI T* opq(T* p) { asm volatile("" : "+s"(p)); return p; }
DEVI int opqs(int a) { asm volatile("" : "+s"(a)); return a; }
DEVI void pin(v4u& a) { asm volatile("" : "+v"(a)); }
DEVI void bar() {
  asm volatile("s_waitcnt lgkmcnt(0)" ::: "memory");
  __builtin_amdgcn_s_barrier();
  asm volatile("" ::: "memory");
}
DEVI void rawbar() {
  asm volatile("" ::: "memory");
  __builtin_amdgcn_s_barrier();
  asm volatile("" ::: "memory");
}

#define TLS(i)

DEVI void publish(char* ws, int grp, int slot, u64 tag, int lane) {
  if (lane == 0) __hip_atomic_store((gu64*)(ws + OFF_FLAG) + grp * 32 + slot, tag, __ATOMIC_RELAXED, __HIP_MEMORY_SCOPE_AGENT);
}
DEVI void wait_flags(char* ws, int grp, int n, u64 tag, int lane) {
  const gu64* p = (const gu64*)(ws + OFF_FLAG) + grp * 32 + (lane & (n - 1));
  for (unsigned spins = 0; spins < (1u << 22); ++spins) {
    u64 v = __hip_atomic_load(p, __ATOMIC_RELAXED, __HIP_MEMORY_SCOPE_AGENT);
    if (__all(v == tag)) break;
    __builtin_amdgcn_s_sleep(1);
  }
  __builtin_amdgcn_fence(__ATOMIC_ACQUIRE, "wavefront");
}

constexpr int GLU_UOFF = 18560, GLU_BUF = 37120;
constexpr int MID_ABYTES = 18 * 1024, MID_BUF = 34 * 1024, MID_NB = 4;
constexpr int LDS_RED = 4 * GLU_BUF;
constexpr int LDS_WDT = 96 * 1024;
constexpr int LDS_TOTAL = LDS_RED + 256;

template <int NP>
DEVI void comb_phase(int mode, unsigned tok0, v4u (&xres)[3], const char* part, char* dstp, unsigned lds0, int w, int lane) {
  const unsigned tok = tok0 + (lane & 7), seg = w * 8 + (lane >> 3);
  if constexpr (NP > 0) {
    const rsrc_t rp = mk_rsrc(part);
    v4u pp[NP][3];
#pragma unroll
    for (int i = 0; i < 3; ++i)
#pragma unroll
      for (int k = 0; k < NP; ++k) pp[k][i] = ld16(rp, k * (unsigned)(SZ_X / 16) + xc_chunk(tok, seg + 24 * i));
#pragma unroll
    for (int i = 0; i < 3; ++i)
#pragma unroll
      for (int j = 0; j < 4; ++j) {
        float lo = 0.f, hi = 0.f;
#pragma unroll
        for (int k = 0; k < NP; ++k) { lo += bflo(pp[k][i][j]); hi += bfhi(pp[k][i][j]); }
        xres[i][j] = pack2(bflo(xres[i][j]) + lo, bfhi(xres[i][j]) + hi);
      }
  }
  const rsrc_t rd = mk_rsrc(dstp);
  float v[3][8];
  float s = 0.f;
#pragma unroll
  for (int i = 0; i < 3; ++i)
#pragma unroll
    for (int j = 0; j < 4; ++j) { v[i][2 * j] = bflo(xres[i][j]); v[i][2 * j + 1] = bfhi(xres[i][j]); s += v[i][2 * j] + v[i][2 * j + 1]; }
  s += __shfl_xor(s, 8); s += __shfl_xor(s, 16); s += __shfl_xor(s, 32);
  const float lm = s * (1.f / 192.f);
  float q = 0.f;
#pragma unroll
  for (int i = 0; i < 3; ++i)
#pragma unroll
    for (int j = 0; j < 8; ++j) { float d = v[i][j] - lm; q += d * d; }
  q += __shfl_xor(q, 8); q += __shfl_xor(q, 16); q += __shfl_xor(q, 32);
  LAS f32x2* red = (LAS f32x2*)(uintptr_t)(lds0 + LDS_RED);
  if (lane < 8) red[w * 8 + lane] = f32x2{lm, q};
  bar();
  if (mode == 2) {
#pragma unroll
    for (int i = 0; i < 3; ++i) st16(rd, tok * 72 + seg + 24 * i, xres[i]);
    return;
  }
  f32x2 a = red[lane & 7], b = red[8 + (lane & 7)], c = red[16 + (lane & 7)];
  const float mean = (a[0] + b[0] + c[0]) * (1.f / 3.f);
  const float da = a[0] - mean, db = b[0] - mean, dc = c[0] - mean;
  const float m2 = a[1] + b[1] + c[1] + 192.f * (da * da + db * db + dc * dc);
  const float rstd = __builtin_amdgcn_rsqf(m2 * (1.f / D) + LN_EPS);
  const float nb = -mean * rstd;
#pragma unroll
  for (int i = 0; i < 3; ++i) {
    unsigned c8 = seg + 24 * i;
    v4u o = {pack2(v[i][0] * rstd + nb, v[i][1] * rstd + nb), pack2(v[i][2] * rstd + nb, v[i][3] * rstd + nb),
             pack2(v[i][4] * rstd + nb, v[i][5] * rstd + nb), pack2(v[i][6] * rstd + nb, v[i][7] * rstd + nb)};
    st16(rd, mode == 1 ? xb_chunk(tok, c8) : xa_chunk(tok, c8), o);
  }
}

DEVI void wdt_unit(const u16* Wd, char* wdt, int cb4, int k32, unsigned stage, int lane) {
  const char* src = (const char*)(Wd + (size_t)(32 * k32) * D + 144 * cb4);
#pragma unroll
  for (int m = 0; m < 9; ++m) {
    unsigned sl = 64 * m + lane;
    unsigned kr = (sl * 3641u) >> 16, ch = sl - 18 * kr;
    glds16<false>(src, kr * (D * 2) + ch * 16, stage + m * 1024);
  }
  wait_vm<0>();
  const int q = lane >> 4, qq = (lane & 15) >> 2, p = lane & 3;
  const unsigned base = stage + (8 * q + qq) * 288;
  const rsrc_t rs = mk_rsrc(wdt);
  const unsigned dst = ((cb4 * 144 + k32) * 9) * 64 + lane;
#pragma unroll
  for (int j = 0; j < 9; ++j) {
    unsigned a = base + (j < 8 ? (64 * (j >> 1) + 16 * p + 8 * (j & 1)) : (256 + 8 * p));
    st16(rs, dst + j * 64, trfrag(a, a + 1152));
  }
}

DEVI void hid_load_w(const u16* Wr, const u16* Wi, int cp, unsigned lds0, int lw, int lane) {
  const int part = cp >> 3, cb4 = (cp >> 1) & 3, half = cp & 1;
  const char* Wb = (const char*)((part ? Wi : Wr) + (size_t)(144 * cb4) * D);
  const unsigned rl = lane >> 3;
#pragma unroll 1
  for (int n = lw; n < 81; n += 4) {
    int st = n / 9, rb = n - 9 * st;
    unsigned key = rb < 8 ? (((rb & 3) << 1) | ((rl >> 1) & 1)) : (rl >> 1);
    unsigned grow = rb < 8 ? (64 * half + 8 * rb + rl) : (128 + 8 * half + rl);
    unsigned voff = grow * 1152 + (((lane & 7) ^ key) << 4);
    glds16<false>(Wb + st * 128, voff, lds0 + n * 1024);
  }
}
DEVI void hid_compute(const char* xa, char* ab, int tt, int cp, unsigned lds0, int w, int lane) {
  const int part = cp >> 3, cb4 = (cp >> 1) & 3, half = cp & 1;
  const int r = lane & 15, q = lane >> 4;
  const int G = 4 * tt + w;
  const char* xbase = xa + (size_t)G * 36 * 1024;
  const unsigned vx = lane * 16;
  v4u xf[18][2];
  auto issue_x = [&](auto kc) {
    constexpr int ks = decltype(kc)::value;
    const char* xp = xbase + (ks >> 1) * 4096;
    gload4<(ks & 1) * 2048>(xf[ks][0], vx, xp);
    gload4<(ks & 1) * 2048 + 1024>(xf[ks][1], vx, xp);
  };
  f32x4 acc[2][5] = {};
  const unsigned rb0 = lds0 + (8 * (r >> 2) + (r & 3)) * 128 + ((q ^ (r >> 1)) << 4);
  const unsigned rb1 = rb0 ^ 64;
  const unsigned re0 = lds0 + (64 + (r & 7)) * 128 + ((q ^ ((r & 7) >> 1)) << 4);
  const unsigned re1 = re0 ^ 64;
  const unsigned rb0h = opaque(rb0 + 4 * 9216), rb1h = opaque(rb1 + 4 * 9216), re0h = opaque(re0 + 4 * 9216), re1h = opaque(re1 + 4 * 9216);
  auto waddr = [&](int i) -> unsigned {
    int ks = i / 5, jj = i % 5;
    unsigned so = ((ks >> 1) & 3) * 9216 + (ks >= 16 ? 4 * 9216 : 0);
    const bool hi = ks >= 8;
    if (jj < 4) return ((ks & 1) ? (hi ? rb1h : rb1) : (hi ? rb0h : rb0)) + so + (32 * (jj >> 1) + 4 * (jj & 1)) * 128;
    return ((ks & 1) ? (hi ? re1h : re1) : (hi ? re0h : re0)) + so;
  };
  constexpr int LA = 5, R = 6, XD = 10;
  v4u wf[R];
  static_for<0, XD>([&](auto kc) { issue_x(kc); });
#pragma unroll
  for (int i = 0; i < LA; ++i) wf[i % R] = lds128(waddr(i));
  static_for<0, 18>([&](auto kc) {
    constexpr int ks = decltype(kc)::value;
    constexpr int younger = (ks + XD <= 18 ? XD - 1 : 17 - ks) * 2;
    wait_vm<younger>();
    pin(xf[ks][0]); pin(xf[ks][1]);
    if constexpr (ks + XD < 18) issue_x(std::integral_constant<int, (ks + XD < 18 ? ks + XD : 0)>{});
#pragma unroll
    for (int jj = 0; jj < 5; ++jj) {
      const int i = 5 * ks + jj;
      if (i + LA < 90) wf[(i + LA) % R] = lds128(waddr(i + LA));
      SB();
      acc[0][jj] = mfma16(xf[ks][0], wf[i % R], acc[0][jj]);
      acc[1][jj] = mfma16(xf[ks][1], wf[i % R], acc[1][jj]);
      SB();
    }
  });
  const float sg = part ? -1.f : 1.f;
  const unsigned dst = ((cb4 * 128 + part * 64 + G) * 9) * 64;
  const rsrc_t rs = mk_rsrc(ab);
#pragma unroll
  for (int j = 0; j < 5; ++j) {
    v4u o = {pack2(sg * acc[0][j][0], sg * acc[0][j][1]), pack2(sg * acc[0][j][2], sg * acc[0][j][3]),
             pack2(sg * acc[1][j][0], sg * acc[1][j][1]), pack2(sg * acc[1][j][2], sg * acc[1][j][3])};
    if (j < 4) st16(rs, dst + (4 * half + j) * 64 + lane, o);
    else if (r < 8) st16(rs, dst + 8 * 64 + 16 * q + 8 * half + r, o);
  }
}

template <bool SEQ> struct MidLoader {
  static constexpr int NS = SEQ ? 16 : 18, NK32 = SEQ ? 128 : 144;
  const char* abase; const char* bbase;
  unsigned voffE, voffO, vl, lds0;
  int lw;
  DEVI void init(const char* b0, const char* b1, const char* at, int kq, int rt, int cb4, unsigned lds0_, int lw_, int lane) {
    lds0 = lds0_; lw = lw_; vl = lane * 16;
    abase = at + ((size_t)(cb4 * NK32 + kq * 2 * NS) * 9) * 1024 + lw * 1024;
    if constexpr (SEQ) {
      bbase = ((kq >> 1) ? b1 : b0) + ((size_t)(128 * rt + 32 * lw) * S + (kq & 1) * 1024) * 2;
      voffE = (lane >> 3) * 4096 + (((lane & 7) ^ (lane >> 4)) << 4);
      voffO = voffE ^ 64;
    } else {
      bbase = b0 + ((size_t)(8 * rt + 2 * lw) * 144 + 36 * kq) * 1024;
      voffE = vl; voffO = vl + 1024;
    }
  }
  DEVI void issue_b(int t) {
    const unsigned bd = lds0 + (t % MID_NB) * MID_BUF + MID_ABYTES + lw * 4096;
    if constexpr (SEQ) {
      const char* cb_ = bbase + t * 128;
#pragma unroll
      for (int n = 0; n < 4; ++n) glds16<false>(cb_ + n * 32768, (n & 1) ? voffO : voffE, bd + n * 1024);
    } else {
      const char* h0 = bbase + t * 2048;
#pragma unroll
      for (int n = 0; n < 4; ++n) glds16<true>(h0 + (n >> 1) * (144 * 1024), (n & 1) ? voffO : voffE, bd + n * 1024);
    }
  }
  DEVI void issue_a(int t) {
    const char* ap = abase + t * 18432;
    const unsigned ld = lds0 + (t % MID_NB) * MID_BUF + lw * 1024;
#pragma unroll
    for (int i = 0; i < 4; ++i) glds16<true>(ap + i * 4096, vl, ld + i * 4096);
    if (lw < 2) glds16<true>(ap + 16384, vl, ld + 16384);
  }
  DEVI void prefetch() {
    if constexpr (SEQ) { issue_b(0); issue_b(1); } else { issue_a(0); issue_a(1); }
  }
  DEVI void run() {
    if constexpr (SEQ) { issue_a(0); issue_a(1); if (lw < 2) wait_vm<5>(); else wait_vm<4>(); }
    else { issue_b(0); issue_b(1); wait_vm<4>(); }
    rawbar();
#pragma unroll 1
    for (int t = 1; t < NS; ++t) {
      if (t + 1 < NS) { issue_b(t + 1); issue_a(t + 1); if (lw < 2) wait_vm<9>(); else wait_vm<8>(); } else wait_vm<0>();
      rawbar();
    }
  }
};
template <bool SEQ>
DEVI void mid_compute(char* part, int kq, int rt, int cb4, unsigned lds0, int w, int lane) {
  constexpr int NS = SEQ ? 16 : 18, NB = MID_NB;
  const int r = lane & 15, q = lane >> 4;
  const unsigned vl = lane * 16;
  unsigned rdb0, rdb1;
  if constexpr (SEQ) {
    rdb0 = lds0 + MID_ABYTES + w * 4096 + r * 128 + ((q ^ (r >> 1)) << 4);
    rdb1 = rdb0 ^ 64;
  } else {
    rdb0 = lds0 + MID_ABYTES + w * 4096 + vl;
    rdb1 = rdb0 + 1024;
  }
  const unsigned rdb2 = opaque(rdb0 + 2 * MID_BUF), rdb3 = opaque(rdb1 + 2 * MID_BUF);
  v4u bf[2][2][2];
  f32x4 acc[9][2] = {};
  constexpr int LA = 6, R = 7;
  v4u af[R];
  const unsigned ra0 = lds0 + vl, ra1 = opaque(ra0 + 2 * MID_BUF);
  auto aaddr = [&](int i) -> unsigned {
    int t = i / 18, f = i % 18, b = t % NB;
    return ((b & 2) ? ra1 : ra0) + (b & 1) * MID_BUF + f * 1024;
  };
  auto sync = [&](int t) {
    rawbar();
    const int b = t % NB;
#pragma unroll
    for (int i = 0; i < 2; ++i) {
      bf[t & 1][i][0] = lds128(((b & 2) ? rdb2 : rdb0) + (b & 1) * MID_BUF + i * 2048);
      bf[t & 1][i][1] = lds128(((b & 2) ? rdb3 : rdb1) + (b & 1) * MID_BUF + i * 2048);
    }
  };
  sync(0);
#pragma unroll
  for (int i = 0; i < LA; ++i) af[i % R] = lds128(aaddr(i));
  static_for<0, NS>([&](auto tc) {
    constexpr int t = decltype(tc)::value;
#pragma unroll
    for (int f = 0; f < 18; ++f) {
      const int i = t * 18 + f;
      if (f == 18 - LA && t + 1 < NS) sync(t + 1);
      if (i + LA < NS * 18) af[(i + LA) % R] = lds128(aaddr(i + LA));
      SB();
      acc[f % 9][0] = mfma16(af[i % R], bf[t & 1][0][f / 9], acc[f % 9][0]);
      acc[f % 9][1] = mfma16(af[i % R], bf[t & 1][1][f / 9], acc[f % 9][1]);
      SB();
    }
  });
  const rsrc_t rs = mk_rsrc(part + (size_t)kq * SZ_X);
#pragma unroll
  for (int i = 0; i < 2; ++i) {
    const unsigned dst = ((8 * rt + 2 * w + i) * 72 + 18 * cb4) * 16;
#pragma unroll
    for (int P = 0; P < 4; ++P) {
      v4u o = {pack2(acc[2 * P][i][0], acc[2 * P][i][1]), pack2(acc[2 * P][i][2], acc[2 * P][i][3]),
               pack2(acc[2 * P + 1][i][0], acc[2 * P + 1][i][1]), pack2(acc[2 * P + 1][i][2], acc[2 * P + 1][i][3])};
      st16(rs, dst + P * 64 + lane, o);
    }
    v2u o2 = {pack2(acc[8][i][0], acc[8][i][1]), pack2(acc[8][i][2], acc[8][i][3])};
    st8(rs, (dst + 256) * 2 + ((q >> 1) * 16 + r) * 2 + (q & 1), o2);
  }
}

DEVI void glu_prefetch(const u16* Wg, const u16* Wu, int cb, unsigned lds0, int lw, int lane) {
#pragma unroll 1
  for (int n = lw; n < 72; n += 4) {
    int t = n >= 36, nn = n - 36 * t;
    int mat = nn >= 18, m = nn - 18 * mat;
    unsigned sl = 64 * m + lane;
    unsigned kr = (sl * 3641u) >> 16, ch = sl - 18 * kr;
    const char* sb = (const char*)((mat ? Wu : Wg) + 144 * cb) + (size_t)t * 64 * F * 2;
    glds16<false>(sb, kr * (F * 2) + ch * 16, lds0 + t * GLU_BUF + mat * GLU_UOFF + m * 1024);
  }
}
template <int DL>
DEVI void glu_body(const char* xb, const u16* Wg, const u16* Wu, char* hb, unsigned lds0, int lane, int w, int G, int cb) {
  unsigned dvo[5];
  const char* dsb[5];
  unsigned dld[5];
#pragma unroll
  for (int i = 0; i < 5; ++i) {
    int n = w + 8 * i; if (n >= 36) n = 35;
    int mat = n >= 18, m = n - 18 * mat;
    unsigned sl = 64 * m + lane;
    unsigned kr = (sl * 3641u) >> 16, ch = sl - 18 * kr;
    dvo[i] = kr * (F * 2) + ch * 16;
    dsb[i] = (const char*)((mat ? Wu : Wg) + 144 * cb);
    dld[i] = lds0 + mat * GLU_UOFF + m * 1024;
  }
  const char* xbase = xb + (size_t)G * 36 * 1024;
  const unsigned vx = lane * 16;
  v4u xf[2][4];
  auto issue_x = [&](auto sc) {
    constexpr int s = decltype(sc)::value;
    const char* xp_ = xbase + s * 4096;
    gload4<0>(xf[s & 1][0], vx, xp_);    gload4<1024>(xf[s & 1][1], vx, xp_);
    gload4<2048>(xf[s & 1][2], vx, xp_); gload4<3072>(xf[s & 1][3], vx, xp_);
  };
  auto issue_d1 = [&](int t, int i) {
    if (i < 4 || DL == 0) glds16<false>(dsb[i] + (size_t)t * 64 * F * 2, dvo[i], dld[i] + (t & 3) * GLU_BUF);
  };
  constexpr int ND = DL == 0 ? 5 : 4;
  const int isu = (lane >> 4) & 1, hf = lane >> 5, qq = (lane & 15) >> 2, p = lane & 3;
  const unsigned tb0 = lds0 + isu * GLU_UOFF + (8 * hf + qq) * 288 + ((((p & 1) << 1) | (p >> 1)) * 8);
  const unsigned tb1 = opaque(tb0 + 2 * GLU_BUF);
  auto waddr = [&](int i) -> unsigned {
    int s = i / 36, ks = (i % 36) / 9, j = i % 9;
    return ((s & 2) ? tb1 : tb0) + (s & 1) * GLU_BUF + ks * 4608 + j * 32;
  };
  f32x16 acc[9];
#pragma unroll
  for (int j = 0; j < 9; ++j) acc[j] = f32x16{};
  constexpr int LA = 4, R = 5;
  v4u wf[R];
  issue_x(std::integral_constant<int, 0>{});
  if constexpr (DL == 1) {
#pragma unroll
    for (int i = 0; i < 4; ++i) issue_d1(2, i);
    wait_vm<ND>();
    pin(xf[0][0]); pin(xf[0][1]); pin(xf[0][2]); pin(xf[0][3]);
    rawbar();
#pragma unroll
    for (int i = 0; i < LA; ++i) wf[i % R] = trfrag(waddr(i), waddr(i) + 1152);
  } else {
#pragma unroll
    for (int i = 0; i < LA; ++i) wf[i % R] = trfrag(waddr(i), waddr(i) + 1152);
    wait_vm<0>();
    pin(xf[0][0]); pin(xf[0][1]); pin(xf[0][2]); pin(xf[0][3]);
  }
  static_for<0, 9>([&](auto sc) {
    constexpr int s = decltype(sc)::value;
    constexpr int t = s + DL;
#pragma unroll
    for (int f = 0; f < 36; ++f) {
      const int i = s * 36 + f;
      if (f == 1 && s + 1 < 9) issue_x(std::integral_constant<int, (s + 1 < 9 ? s + 1 : 0)>{});
      if (t + 2 < 9 && f >= 5 && f <= 21 && (f & 3) == 1) issue_d1(t + 2, (f - 5) >> 2);
      if (f == 36 - LA && s + 1 < 9) {
        if constexpr (t + 2 < 9) wait_vm<ND>(); else wait_vm<0>();
        pin(xf[(s + 1) & 1][0]); pin(xf[(s + 1) & 1][1]); pin(xf[(s + 1) & 1][2]); pin(xf[(s + 1) & 1][3]);
        if constexpr (t + 1 < 9) {
          if constexpr (DL == 1) bar(); else rawbar();
        }
      }
      if (i + LA < 324) wf[(i + LA) % R] = trfrag(waddr(i + LA), waddr(i + LA) + 1152);
      SB();
      acc[f % 9] = mfma32(wf[i % R], xf[s & 1][f / 9], acc[f % 9]);
      SB();
    }
  });
  const unsigned tok = 32 * G + (lane & 31);
  const unsigned hdst = (tok >> 4) * 144 * 64 + (tok & 15) + hf * 16;
  const rsrc_t rs = mk_rsrc(hb);
#pragma unroll
  for (int j = 0; j < 9; ++j) {
    unsigned ow[4];
#pragma unroll
    for (int i = 0; i < 4; ++i) {
      f32x2 g = {acc[j][2 * i], acc[j][2 * i + 1]};
      f32x2 u = {acc[j][8 + 2 * i], acc[j][9 + 2 * i]};
      g = g * f32x2{-1.44269504088896340736f, -1.44269504088896340736f};
      f32x2 e = {__builtin_amdgcn_exp2f(g[0]), __builtin_amdgcn_exp2f(g[1])};
      e = e + f32x2{1.f, 1.f};
      f32x2 rc = {__builtin_amdgcn_rcpf(e[0]), __builtin_amdgcn_rcpf(e[1])};
      u = u * rc;
      ow[i] = pack2(u[0], u[1]);
    }
    v4u o = {ow[0], ow[1], ow[2], ow[3]};
    unsigned J = 9 * cb + j;
    st16(rs, hdst + (J >> 1) * 64 + (J & 1) * 32, o);
  }
}

__global__ __launch_bounds__(512, 2) void k_all(const u16* __restrict__ x0, const char* __restrict__ Cr, const char* __restrict__ Ci,
                                                const u16* __restrict__ Wr, const u16* __restrict__ Wi, const u16* __restrict__ Wg,
                                                const u16* __restrict__ Wu, const u16* __restrict__ Wd, char* ws, char* out) {
  extern __shared__ __attribute__((aligned(16))) char smem[];
  const int lane = threadIdx.x & 63;
  const int w = __builtin_amdgcn_readfirstlane(threadIdx.x >> 6);
  const int lw = w & 3;
  const int g8 = blockIdx.x & 7, idx = blockIdx.x >> 3;
  const int kq = g8 >> 1, rt = 8 * (g8 & 1) + (idx >> 2), cb4 = idx & 3, j16 = kq * 4 + cb4;
  const int panel = (g8 & 1) * 4 + (idx >> 3), cb = (g8 >> 1) * 8 + (idx & 7);
  const unsigned lds0 = (unsigned)(uintptr_t)(LAS char*)smem;
  const unsigned tok0 = 128 * rt + 8 * j16;
  TLS(0);
  u64 tag = 0;
  if (w == 4) tag = __hip_atomic_load((gu64*)(ws + OFF_FLAG), __ATOMIC_RELAXED, __HIP_MEMORY_SCOPE_AGENT) + 1;
  v4u xres[3] = {};
  if (w < 3) {
    const unsigned tok = tok0 + (lane & 7), seg = w * 8 + (lane >> 3);
#pragma unroll
    for (int i = 0; i < 3; ++i) xres[i] = ((const v4u*)x0)[tok * 72 + seg + 24 * i];
    comb_phase<0>(0, tok0, xres, nullptr, ws + OFF_XN, lds0, w, lane);
    wait_vm<0>();
  } else {
    if (w >= 4) hid_load_w(Wr, Wi, j16, lds0, lw, lane);
    rawbar();
  }
  rawbar();
  if (w == 4) publish(ws, FG_A + rt, j16, tag, lane);
#pragma unroll 1
  for (int l = 0; l < 2; ++l) {
    const int fb = l * FG_LAYER;
    char* ab = ws + OFF_AB + l * SZ_AB;
    char* hb = ws + OFF_H + l * SZ_H;
    char* p0 = ws + OFF_P + (2 * l) * SZ_P;
    char* p1 = p0 + SZ_P;
    char* xna = ws + OFF_XN + (2 * l) * SZ_X;
    char* xnb = xna + SZ_X;
    TLS(1 + 12 * l);
    if (w == 4) wait_flags(ws, fb + FG_A + rt, 16, tag, lane);
    if (w >= 4) wait_vm<0>();
    rawbar();
    TLS(2 + 12 * l);
    if (w < 4) hid_compute(opq(xna), opq(ab), opqs(rt), opqs(j16), (unsigned)opqs(lds0), opqs(w), opaque(lane));
    else if (l == 0 && w >= 5 && rt < 12) wdt_unit(opq(Wd), opq(ws) + OFF_WDT, cb4, 36 * kq + 3 * rt + (w - 5), lds0 + LDS_WDT + (w - 5) * 9216, opaque(lane));
    wait_vm<0>();
    bar();
    TLS(3 + 12 * l);
    if (w >= 4) {
      if (w == 4) publish(ws, fb + FG_B + (j16 >> 3) * 8 + (rt >> 3) * 4 + ((j16 >> 1) & 3), (rt & 7) * 2 + (j16 & 1), tag, lane);
      MidLoader<true> ld;
      ld.init(opq(Cr), opq(Ci), opq(ab), opqs(kq), opqs(rt), opqs(cb4), (unsigned)opqs(lds0), opqs(lw), opaque(lane));
      ld.prefetch();
      if (w == 4) wait_flags(ws, fb + FG_B + kq * 4 + cb4, 16, tag, lane);
      rawbar();
      TLS(4 + 12 * l);
      ld.run();
    } else {
      rawbar();
      mid_compute<true>(opq(p0), opqs(kq), opqs(rt), opqs(cb4), (unsigned)opqs(lds0), opqs(w), opaque(lane));
      wait_vm<0>();
    }
    bar();
    TLS(5 + 12 * l);
    if (w == 4) publish(ws, fb + FG_C + rt, j16, tag, lane);
    if (w >= 4) glu_prefetch(opq(Wg), opq(Wu), opqs(cb), (unsigned)opqs(lds0), opqs(lw), opaque(lane));
    if (w == 4) wait_flags(ws, fb + FG_C + rt, 16, tag, lane);
    rawbar();
    TLS(6 + 12 * l);
    if (w < 3) { comb_phase<4>(1, opqs(tok0), xres, opq(p0), opq(xnb), (unsigned)opqs(lds0), opqs(w), opaque(lane)); wait_vm<0>(); }
    else rawbar();
    rawbar();
    TLS(7 + 12 * l);
    if (w == 4) {
      publish(ws, fb + FG_D + (rt >> 1), (rt & 1) * 16 + j16, tag, lane);
      wait_flags(ws, fb + FG_D + panel, 32, tag, lane);
    }
    if (w >= 4) wait_vm<0>();
    rawbar();
    TLS(8 + 12 * l);
    if (w < 4) glu_body<0>(opq(xnb), opq(Wg), opq(Wu), opq(hb), (unsigned)opqs(lds0), opaque(lane), opqs(w), opqs(panel * 8 + w), opqs(cb));
    else glu_body<1>(opq(xnb), opq(Wg), opq(Wu), opq(hb), (unsigned)opqs(lds0), opaque(lane), opqs(w), opqs(panel * 8 + w), opqs(cb));
    wait_vm<0>();
    bar();
    TLS(9 + 12 * l);
    if (w >= 4) {
      if (w == 4) publish(ws, fb + FG_E + panel * 4 + (cb >> 3), cb & 7, tag, lane);
      MidLoader<false> ld;
      ld.init(opq(hb), nullptr, opq(ws) + OFF_WDT, opqs(kq), opqs(rt), opqs(cb4), (unsigned)opqs(lds0), opqs(lw), opaque(lane));
      ld.prefetch();
      if (w == 4) wait_flags(ws, fb + FG_E + (rt >> 1) * 4 + kq, 8, tag, lane);
      rawbar();
      TLS(10 + 12 * l);
      ld.run();
    } else {
      rawbar();
      mid_compute<false>(opq(p1), opqs(kq), opqs(rt), opqs(cb4), (unsigned)opqs(lds0), opqs(w), opaque(lane));
      wait_vm<0>();
    }
    bar();
    TLS(11 + 12 * l);
    if (w == 4) publish(ws, fb + FG_F + rt, j16, tag, lane);
    if (w >= 4 && l == 0) hid_load_w(opq(Wr), opq(Wi), opqs(j16), (unsigned)opqs(lds0), opqs(lw), opaque(lane));
    if (w == 4) wait_flags(ws, fb + FG_F + rt, 16, tag, lane);
    rawbar();
    TLS(12 + 12 * l);
    if (w < 3) { comb_phase<4>(l == 0 ? 0 : 2, opqs(tok0), xres, opq(p1), opq(l == 0 ? xnb + SZ_X : out), (unsigned)opqs(lds0), opqs(w), opaque(lane)); wait_vm<0>(); }
    else rawbar();
    rawbar();
    if (w == 4 && l == 0) publish(ws, FG_LAYER + FG_A + rt, j16, tag, lane);
  }
  TLS(25);
  if (blockIdx.x == 0 && w == 4 && lane == 0)
    __hip_atomic_store((gu64*)(ws + OFF_FLAG), tag, __ATOMIC_RELAXED, __HIP_MEMORY_SCOPE_AGENT);
}

extern "C" void kernel_launch(void* const* d_in, const int* in_sizes, int n_in, void* d_out, int out_size, void* d_ws,
                              size_t ws_size, hipStream_t stream) {
  (void)hipFuncSetAttribute((const void*)k_all, hipFuncAttributeMaxDynamicSharedMemorySize, LDS_TOTAL);
  k_all<<<256, 512, LDS_TOTAL, stream>>>((const u16*)d_in[0], (const char*)d_in[1], (const char*)d_in[2], (const u16*)d_in[3],
                                         (const u16*)d_in[4], (const u16*)d_in[6], (const u16*)d_in[7], (const u16*)d_in[8],
                                         (char*)d_ws, (char*)d_out);
}
```

```cpp
#include <hip/hip_runtime.h>
#include <hip/hip_bf16.h>
#include <stdint.h>
#include <type_traits>

typedef unsigned short u16;
typedef unsigned long long u64;
typedef unsigned v4u __attribute__((ext_vector_type(4)));
typedef unsigned v2u __attribute__((ext_vector_type(2)));
typedef short v4s __attribute__((ext_vector_type(4)));
typedef float f32x2 __attribute__((ext_vector_type(2)));
typedef float f32x4 __attribute__((ext_vector_type(4)));
typedef float f32x16 __attribute__((ext_vector_type(16)));
typedef __bf16 bf16x2 __attribute__((ext_vector_type(2)));
typedef __bf16 bf16x8 __attribute__((ext_vector_type(8)));
#define GAS __attribute__((address_space(1)))
#define LAS __attribute__((address_space(3)))
#define DEVI __device__ __forceinline__
typedef GAS u64 gu64;

constexpr int S = 2048, D = 576, F = 4608;
constexpr float LN_EPS = 1e-5f;

constexpr size_t SZ_X = (size_t)S * D * 2;
constexpr size_t SZ_AB = 2 * SZ_X;
constexpr size_t SZ_H = (size_t)S * F * 2;
constexpr size_t SZ_WDT = (size_t)F * D * 2;
constexpr size_t SZ_P = 4 * SZ_X;
constexpr size_t OFF_FLAG = 0;
constexpr size_t OFF_XN = 1048576;
constexpr size_t OFF_AB = OFF_XN + 4 * SZ_X;
constexpr size_t OFF_P = OFF_AB + 2 * SZ_AB;
constexpr size_t OFF_H = OFF_P + 4 * SZ_P;
constexpr size_t OFF_WDT = OFF_H + 2 * SZ_H;
constexpr size_t OFF_WGU = OFF_WDT + SZ_WDT;
constexpr size_t WS_END = OFF_WGU + 2 * SZ_WDT;
constexpr int FST = 8;
constexpr int FG_A = 1, FG_B = FG_A + 16, FG_C = FG_B + 16, FG_D = FG_C + 16, FG_E = FG_D + 8, FG_F = FG_E + 32, FG_LAYER = 104, FG_W = 1 + 2 * FG_LAYER;

DEVI float u2f(unsigned u) { return __builtin_bit_cast(float, u); }
DEVI float bflo(unsigned u) { return u2f(u << 16); }
DEVI float bfhi(unsigned u) { return u2f(u & 0xffff0000u); }
DEVI unsigned pack2(float a, float b) {
  f32x2 v = {a, b};
  return __builtin_bit_cast(unsigned, __builtin_convertvector(v, bf16x2));
}
DEVI f32x4 mfma16(v4u a, v4u b, f32x4 c) {
  return __builtin_amdgcn_mfma_f32_16x16x32_bf16(__builtin_bit_cast(bf16x8, a), __builtin_bit_cast(bf16x8, b), c, 0, 0, 0);
}
DEVI f32x16 mfma32(v4u a, v4u b, f32x16 c) {
  return __builtin_amdgcn_mfma_f32_32x32x16_bf16(__builtin_bit_cast(bf16x8, a), __builtin_bit_cast(bf16x8, b), c, 0, 0, 0);
}
DEVI v4u lds128(unsigned addr) { return *(const LAS v4u*)(uintptr_t)addr; }
DEVI v4u trfrag(unsigned a0, unsigned a1) {
  v4s x = __builtin_amdgcn_ds_read_tr16_b64_v4i16((LAS v4s*)(uintptr_t)a0);
  v4s y = __builtin_amdgcn_ds_read_tr16_b64_v4i16((LAS v4s*)(uintptr_t)a1);
  return __builtin_bit_cast(v4u, __builtin_shufflevector(x, y, 0, 1, 2, 3, 4, 5, 6, 7));
}
DEVI unsigned xa_chunk(unsigned s, unsigned c8) {
  unsigned G = s >> 5, u = s & 31;
  unsigned r = 4 * (u >> 3) + (u & 3), t = (u >> 2) & 1;
  return ((G * 18 + (c8 >> 2)) * 2 + t) * 64 + (c8 & 3) * 16 + r;
}
DEVI unsigned xb_chunk(unsigned s, unsigned c8) {
  return ((s >> 4) * 18 + (c8 >> 2)) * 64 + (c8 & 3) * 16 + (s & 15);
}
DEVI unsigned xc_chunk(unsigned s, unsigned c8) {
  return ((s >> 4) * 72 + c8) * 16 + (s & 15);
}
template <bool SC1> DEVI void glds16(const void* sbase, unsigned voff, unsigned ldsdst) {
  unsigned keep;
  if constexpr (SC1)
    asm volatile("s_mov_b32 %0, m0\n\ts_mov_b32 m0, %3\n\ts_nop 0\n\tglobal_load_lds_dwordx4 %1, %2 sc1\n\ts_mov_b32 m0, %0"
                 : "=&s"(keep) : "v"(voff), "s"(sbase), "s"(ldsdst) : "memory");
  else
    asm volatile("s_mov_b32 %0, m0\n\ts_mov_b32 m0, %3\n\ts_nop 0\n\tglobal_load_lds_dwordx4 %1, %2\n\ts_mov_b32 m0, %0"
                 : "=&s"(keep) : "v"(voff), "s"(sbase), "s"(ldsdst) : "memory");
}
template <bool SC1> DEVI void glds16x4(const void* sbase, unsigned v0, unsigned v1, unsigned v2, unsigned v3, unsigned ldsdst) {
  unsigned keep;
  if constexpr (SC1)
    asm volatile("s_mov_b32 %0, m0\n\ts_mov_b32 m0, %6\n\ts_nop 0\n\tglobal_load_lds_dwordx4 %1, %5 sc1\n\t"
                 "global_load_lds_dwordx4 %2, %5 offset:1024 sc1\n\tglobal_load_lds_dwordx4 %3, %5 offset:2048 sc1\n\t"
                 "global_load_lds_dwordx4 %4, %5 offset:3072 sc1\n\ts_mov_b32 m0, %0"
                 : "=&s"(keep) : "v"(v0), "v"(v1), "v"(v2), "v"(v3), "s"(sbase), "s"(ldsdst) : "memory");
  else
    asm volatile("s_mov_b32 %0, m0\n\ts_mov_b32 m0, %6\n\ts_nop 0\n\tglobal_load_lds_dwordx4 %1, %5\n\t"
                 "global_load_lds_dwordx4 %2, %5 offset:1024\n\tglobal_load_lds_dwordx4 %3, %5 offset:2048\n\t"
                 "global_load_lds_dwordx4 %4, %5 offset:3072\n\ts_mov_b32 m0, %0"
                 : "=&s"(keep) : "v"(v0), "v"(v1), "v"(v2), "v"(v3), "s"(sbase), "s"(ldsdst) : "memory");
}
template <int IMM> DEVI void gload4(v4u& dst, unsigned voff, const void* sbase) {
  asm volatile("global_load_dwordx4 %0, %1, %2 offset:%3 sc1" : "=v"(dst) : "v"(voff), "s"(sbase), "n"(IMM) : "memory");
}
typedef __amdgpu_buffer_rsrc_t rsrc_t;
DEVI rsrc_t mk_rsrc(const void* p) { return __builtin_amdgcn_make_buffer_rsrc(const_cast<void*>(p), 0, 0x7fffffff, 0x00020000); }
DEVI void st16(rsrc_t rs, unsigned chunk, v4u v) { __builtin_amdgcn_raw_buffer_store_b128(v, rs, chunk * 16, 0, 16); }
DEVI void st8(rsrc_t rs, unsigned off8, v2u v) { __builtin_amdgcn_raw_buffer_store_b64(v, rs, off8 * 8, 0, 16); }
DEVI v4u ld16(rsrc_t rs, unsigned chunk) { return __builtin_amdgcn_raw_buffer_load_b128(rs, chunk * 16, 0, 16); }
#define SB() __builtin_amdgcn_sched_barrier(0)
template <int N> DEVI void wait_vm() { asm volatile("s_waitcnt vmcnt(%0)" ::"n"(N) : "memory"); }
template <int I, int N, class Fn> DEVI void static_for(Fn&& fn) {
  if constexpr (I < N) { fn(std::integral_constant<int, I>{}); static_for<I + 1, N>(fn); }
}
DEVI unsigned opaque(unsigned a) { asm volatile("" : "+v"(a)); return a; }
template <class T> DEVI T* opq(T* p) { asm volatile("" : "+s"(p)); return p; }
DEVI int opqs(int a) { asm volatile("" : "+s"(a)); return a; }
DEVI void pin(v4u& a) { asm volatile("" : "+v"(a)); }
DEVI void bar() {
  asm volatile("s_waitcnt lgkmcnt(0)" ::: "memory");
  __builtin_amdgcn_s_barrier();
  asm volatile("" ::: "memory");
}
DEVI void rawbar() {
  asm volatile("" ::: "memory");
  __builtin_amdgcn_s_barrier();
  asm volatile("" ::: "memory");
}

DEVI void publish(char* ws, int grp, int slot, u64 tag, int lane) {
  if (lane == 0) __hip_atomic_store((gu64*)(ws + OFF_FLAG) + (grp * 32 + slot) * FST, tag, __ATOMIC_RELAXED, __HIP_MEMORY_SCOPE_AGENT);
}
DEVI void wait_flags(char* ws, int grp, int n, u64 tag, int lane, int slot0 = 0) {
  const gu64* p = (const gu64*)(ws + OFF_FLAG) + (grp * 32 + slot0 + (lane & (n - 1))) * FST;
#pragma unroll 1
  for (unsigned spins = 0; spins < (1u << 20); ++spins) {
    u64 v = __hip_atomic_load(p, __ATOMIC_RELAXED, __HIP_MEMORY_SCOPE_AGENT);
    if (__all(v == tag)) break;
    __builtin_amdgcn_s_sleep(1);
  }
  __builtin_amdgcn_fence(__ATOMIC_ACQUIRE, "wavefront");
}

constexpr int GLU_BUF = 20 * 1024, GLU_NB = 6;
constexpr int MID_ABYTES = 18 * 1024, MID_BUF = 34 * 1024, MID_NB = 4;
constexpr int LDS_RED = MID_NB * MID_BUF;
constexpr int LDS_WDT = 84 * 1024;
constexpr int LDS_TOTAL = LDS_RED + 256;
constexpr float NLOG2E = -1.44269504088896340736f, NLN2 = -0.69314718055994530942f;

template <int NP>
DEVI void comb_phase(int mode, unsigned tok0, v4u (&xres)[3], const char* part, char* dstp, unsigned lds0, int w, int lane) {
  const unsigned tok = tok0 + (lane & 7), seg = w * 8 + (lane >> 3);
  if constexpr (NP > 0) {
    const rsrc_t rp = mk_rsrc(part);
    v4u pp[NP][3];
#pragma unroll
    for (int i = 0; i < 3; ++i)
#pragma unroll
      for (int k = 0; k < NP; ++k) pp[k][i] = ld16(rp, k * (unsigned)(SZ_X / 16) + xc_chunk(tok, seg + 24 * i));
#pragma unroll
    for (int i = 0; i < 3; ++i)
#pragma unroll
      for (int j = 0; j < 4; ++j) {
        float lo = 0.f, hi = 0.f;
#pragma unroll
        for (int k = 0; k < NP; ++k) { lo += bflo(pp[k][i][j]); hi += bfhi(pp[k][i][j]); }
        xres[i][j] = pack2(bflo(xres[i][j]) + lo, bfhi(xres[i][j]) + hi);
      }
  }
  const rsrc_t rd = mk_rsrc(dstp);
  float v[3][8];
  float s = 0.f;
#pragma unroll
  for (int i = 0; i < 3; ++i)
#pragma unroll
    for (int j = 0; j < 4; ++j) { v[i][2 * j] = bflo(xres[i][j]); v[i][2 * j + 1] = bfhi(xres[i][j]); s += v[i][2 * j] + v[i][2 * j + 1]; }
  s += __shfl_xor(s, 8); s += __shfl_xor(s, 16); s += __shfl_xor(s, 32);
  const float lm = s * (1.f / 192.f);
  float q = 0.f;
#pragma unroll
  for (int i = 0; i < 3; ++i)
#pragma unroll
    for (int j = 0; j < 8; ++j) { float d = v[i][j] - lm; q += d * d; }
  q += __shfl_xor(q, 8); q += __shfl_xor(q, 16); q += __shfl_xor(q, 32);
  LAS f32x2* red = (LAS f32x2*)(uintptr_t)(lds0 + LDS_RED);
  if (lane < 8) red[w * 8 + lane] = f32x2{lm, q};
  bar();
  if (mode == 2) {
#pragma unroll
    for (int i = 0; i < 3; ++i) st16(rd, tok * 72 + seg + 24 * i, xres[i]);
    return;
  }
  f32x2 a = red[lane & 7], b = red[8 + (lane & 7)], c = red[16 + (lane & 7)];
  const float mean = (a[0] + b[0] + c[0]) * (1.f / 3.f);
  const float da = a[0] - mean, db = b[0] - mean, dc = c[0] - mean;
  const float m2 = a[1] + b[1] + c[1] + 192.f * (da * da + db * db + dc * dc);
  float rstd = __builtin_amdgcn_rsqf(m2 * (1.f / D) + LN_EPS);
  if (mode == 1) rstd *= NLOG2E;
  const float nb = -mean * rstd;
#pragma unroll
  for (int i = 0; i < 3; ++i) {
    unsigned c8 = seg + 24 * i;
    v4u o = {pack2(v[i][0] * rstd + nb, v[i][1] * rstd + nb), pack2(v[i][2] * rstd + nb, v[i][3] * rstd + nb),
             pack2(v[i][4] * rstd + nb, v[i][5] * rstd + nb), pack2(v[i][6] * rstd + nb, v[i][7] * rstd + nb)};
    st16(rd, mode == 1 ? xb_chunk(tok, c8) : xa_chunk(tok, c8), o);
  }
}

struct Retile {
  unsigned dst;
  int kind;
  DEVI void issue(int unit, const u16* Wg, const u16* Wu, const u16* Wd, unsigned stage, int lane) {
    kind = unit < 0 ? -1 : (unit < 1152 ? 0 : 1);
    if (unit < 0) return;
    if (unit < 1152) {
      const int cb = unit / 36, rem = unit - 36 * cb, k32 = rem >> 1, mat = rem & 1;
      const char* src = (const char*)((mat ? Wu : Wg) + (size_t)(32 * k32) * F + 144 * cb);
#pragma unroll
      for (int m = 0; m < 9; ++m) {
        unsigned sl = 64 * m + lane;
        unsigned kr = (sl * 3641u) >> 16, ch = sl - 18 * kr;
        glds16<false>(src, kr * (F * 2) + ch * 16, stage + m * 1024);
      }
      dst = cb * (324 * 64) + mat * 64 + k32;
    } else {
      const int u = unit - 1152, cb4 = u / 144, k32 = u - 144 * cb4, kq = k32 / 36, uu = k32 - 36 * kq;
      const char* src = (const char*)(Wd + (size_t)(1152 * kq) * D + 144 * cb4);
#pragma unroll
      for (int m = 0; m < 9; ++m) {
        unsigned sl = 64 * m + lane;
        unsigned kr = (sl * 3641u) >> 16, ch = sl - 18 * kr;
        unsigned c = 4 * uu + (kr >> 3);
        unsigned f;
        if (c < 80) { unsigned cbl = (c * 205u) >> 11, id = c - 10 * cbl; f = 144 * cbl + (id < 8 ? 8 * id : 64 + 8 * id); }
        else { unsigned c2 = c - 80; f = 144 * (c2 >> 3) + 64 + 8 * (c2 & 7); }
        glds16<false>(src, (f + (kr & 7)) * (D * 2) + ch * 16, stage + m * 1024);
      }
      dst = ((cb4 * 144 + k32) * 9) * 64;
    }
  }
  DEVI void finish(char* ws, unsigned stage, int lane) {
    if (kind < 0) return;
    const int q = lane >> 4, qq = (lane & 15) >> 2, p = lane & 3;
    const unsigned base = stage + (8 * q + qq) * 288;
    const rsrc_t rs = mk_rsrc(ws + (kind ? OFF_WDT : OFF_WGU));
    const unsigned k32 = dst & 63, b0 = dst & ~63u;
#pragma unroll
    for (int j = 0; j < 9; ++j) {
      unsigned a = base + (j < 8 ? (64 * (j >> 1) + 16 * p + 8 * (j & 1)) : (256 + 8 * p));
      unsigned d;
      if (kind) d = dst + j * 64;
      else if (j >= 4 && j < 8) d = b0 + (180 + 8 * k32 + 2 * (j - 4)) * 64;
      else d = b0 + (10 * k32 + 2 * (j < 4 ? j : 4)) * 64;
      st16(rs, d + lane, trfrag(a, a + 1152));
    }
  }
};

DEVI void hid_load_w(const u16* Wr, const u16* Wi, int cp, unsigned lds0, int lw, int lane) {
  const int part = cp >> 3, cb4 = (cp >> 1) & 3, half = cp & 1;
  const char* Wb = (const char*)((part ? Wi : Wr) + (size_t)(144 * cb4) * D);
  const unsigned rl = lane >> 3;
#pragma unroll 1
  for (int n = lw; n < 81; n += 4) {
    int st = n / 9, rb = n - 9 * st;
    unsigned key = rb < 8 ? (((rb & 3) << 1) | ((rl >> 1) & 1)) : (rl >> 1);
    unsigned grow = rb < 8 ? (64 * half + 8 * rb + rl) : (128 + 8 * half + rl);
    unsigned voff = grow * 1152 + (((lane & 7) ^ key) << 4);
    glds16<false>(Wb + st * 128, voff, lds0 + n * 1024);
  }
}
DEVI void hid_compute(const char* xa, char* ab, int tt, int cp, unsigned lds0, int w, int lane) {
  const int part = cp >> 3, cb4 = (cp >> 1) & 3, half = cp & 1;
  const int r = lane & 15, q = lane >> 4;
  const int G = 4 * tt + w;
  const char* xbase = xa + (size_t)G * 36 * 1024;
  const unsigned vx = lane * 16;
  v4u xf[18][2];
  auto issue_x = [&](auto kc) {
    constexpr int ks = decltype(kc)::value;
    const char* xp = xbase + (ks >> 1) * 4096;
    gload4<(ks & 1) * 2048>(xf[ks][0], vx, xp);
    gload4<(ks & 1) * 2048 + 1024>(xf[ks][1], vx, xp);
  };
  f32x4 acc[2][5] = {};
  const unsigned rb0 = lds0 + (8 * (r >> 2) + (r & 3)) * 128 + ((q ^ (r >> 1)) << 4);
  const unsigned rb1 = rb0 ^ 64;
  const unsigned re0 = lds0 + (64 + (r & 7)) * 128 + ((q ^ ((r & 7) >> 1)) << 4);
  const unsigned re1 = re0 ^ 64;
  const unsigned rb0h = opaque(rb0 + 4 * 9216), rb1h = opaque(rb1 + 4 * 9216), re0h = opaque(re0 + 4 * 9216), re1h = opaque(re1 + 4 * 9216);
  auto waddr = [&](int i) -> unsigned {
    int ks = i / 5, jj = i % 5;
    unsigned so = ((ks >> 1) & 3) * 9216 + (ks >= 16 ? 4 * 9216 : 0);
    const bool hi = ks >= 8;
    if (jj < 4) return ((ks & 1) ? (hi ? rb1h : rb1) : (hi ? rb0h : rb0)) + so + (32 * (jj >> 1) + 4 * (jj & 1)) * 128;
    return ((ks & 1) ? (hi ? re1h : re1) : (hi ? re0h : re0)) + so;
  };
  constexpr int LA = 5, R = 6, XD = 10;
  v4u wf[R];
  static_for<0, XD>([&](auto kc) { issue_x(kc); });
#pragma unroll
  for (int i = 0; i < LA; ++i) wf[i % R] = lds128(waddr(i));
  static_for<0, 18>([&](auto kc) {
    constexpr int ks = decltype(kc)::value;
    constexpr int younger = (ks + XD <= 18 ? XD - 1 : 17 - ks) * 2;
    wait_vm<younger>();
    pin(xf[ks][0]); pin(xf[ks][1]);
    if constexpr (ks + XD < 18) issue_x(std::integral_constant<int, (ks + XD < 18 ? ks + XD : 0)>{});
#pragma unroll
    for (int jj = 0; jj < 5; ++jj) {
      const int i = 5 * ks + jj;
      if (i + LA < 90) wf[(i + LA) % R] = lds128(waddr(i + LA));
      SB();
      acc[0][jj] = mfma16(xf[ks][0], wf[i % R], acc[0][jj]);
      acc[1][jj] = mfma16(xf[ks][1], wf[i % R], acc[1][jj]);
      SB();
    }
  });
  const float sg = part ? -1.f : 1.f;
  const unsigned dst = ((cb4 * 128 + part * 64 + G) * 9) * 64;
  const rsrc_t rs = mk_rsrc(ab);
#pragma unroll
  for (int j = 0; j < 5; ++j) {
    v4u o = {pack2(sg * acc[0][j][0], sg * acc[0][j][1]), pack2(sg * acc[0][j][2], sg * acc[0][j][3]),
             pack2(sg * acc[1][j][0], sg * acc[1][j][1]), pack2(sg * acc[1][j][2], sg * acc[1][j][3])};
    if (j < 4) st16(rs, dst + (4 * half + j) * 64 + lane, o);
    else if (r < 8) st16(rs, dst + 8 * 64 + 16 * q + 8 * half + r, o);
  }
}

template <bool SEQ> struct MidLoader {
  static constexpr int NS = SEQ ? 16 : 18, NK32 = SEQ ? 128 : 144;
  const char* abase; const char* bbase;
  unsigned vb[4], vl, lds0;
  int lw;
  DEVI void init(const char* b0, const char* b1, const char* at, int kq, int rt, int cb4, unsigned lds0_, int lw_, int lane) {
    lds0 = lds0_; lw = lw_; vl = lane * 16;
    abase = at + ((size_t)(cb4 * NK32 + kq * 2 * NS) * 9) * 1024;
    if constexpr (SEQ) {
      bbase = ((kq >> 1) ? b1 : b0) + ((size_t)(128 * rt + 32 * lw) * S + (kq & 1) * 1024) * 2;
      const unsigned voffE = (lane >> 3) * 4096 + (((lane & 7) ^ (lane >> 4)) << 4);
#pragma unroll
      for (int n = 0; n < 4; ++n) vb[n] = ((n & 1) ? (voffE ^ 64) : voffE) + n * (32768 - 1024);
    } else {
      bbase = b0 + ((size_t)(8 * rt + 2 * lw) * 144 + 36 * kq) * 1024;
      vb[0] = vb[1] = vl; vb[2] = vb[3] = vl + 144 * 1024 - 2048;
    }
  }
  DEVI void issue_b(int t) {
    const unsigned bd = lds0 + (t % MID_NB) * MID_BUF + MID_ABYTES + lw * 4096;
    glds16x4<!SEQ>(bbase + t * (SEQ ? 128 : 2048), vb[0], vb[1], vb[2], vb[3], bd);
  }
  DEVI void issue_a(int t) {
    const char* ap = abase + t * 18432;
    const unsigned ld = lds0 + (t % MID_NB) * MID_BUF;
    glds16x4<true>(ap + lw * 4096, vl, vl, vl, vl, ld + lw * 4096);
    if (lw < 2) glds16<true>(ap + 16384 + lw * 1024, vl, ld + 16384 + lw * 1024);
  }
  template <bool CONST_OP> DEVI void issue_op(int t) { if constexpr (CONST_OP == SEQ) issue_b(t); else issue_a(t); }
  DEVI void wait_dep1() {
    if constexpr (SEQ) { if (lw < 2) wait_vm<5>(); else wait_vm<4>(); } else wait_vm<4>();
  }
  static constexpr int PFD = SEQ ? 4 : 1;
  DEVI void prefetch() {
#pragma unroll
    for (int t = 0; t < PFD; ++t) issue_op<true>(t);
  }
  DEVI void wait_full1() { if (lw < 2) wait_vm<9>(); else wait_vm<8>(); }
  template <class Hook> DEVI void run(Hook&& hook) {
    if constexpr (PFD == 4) {
      issue_op<false>(0); issue_op<false>(1);
      wait_dep1(); rawbar();
      hook(0);
      issue_op<false>(2); wait_dep1(); rawbar();
      issue_op<false>(3); wait_dep1(); rawbar();
    } else {
      issue_op<false>(0); issue_b(1); issue_a(1);
      wait_full1(); rawbar();
      hook(0);
      issue_b(2); issue_a(2); wait_full1(); rawbar();
      issue_b(3); issue_a(3); wait_full1(); rawbar();
    }
#pragma unroll 1
    for (int t = 3; t < NS; ++t) {
      if (t + 1 < NS) { issue_b(t + 1); issue_a(t + 1); wait_full1(); } else wait_vm<0>();
      hook(t);
      rawbar();
    }
  }
};
template <bool SEQ>
DEVI void mid_compute(char* part, int kq, int rt, int cb4, unsigned lds0, int w, int lane) {
  constexpr int NS = SEQ ? 16 : 18, NB = MID_NB;
  const int r = lane & 15, q = lane >> 4;
  const unsigned vl = lane * 16;
  unsigned rdb0, rdb1;
  if constexpr (SEQ) {
    rdb0 = lds0 + MID_ABYTES + w * 4096 + r * 128 + ((q ^ (r >> 1)) << 4);
    rdb1 = rdb0 ^ 64;
  } else {
    rdb0 = lds0 + MID_ABYTES + w * 4096 + vl;
    rdb1 = rdb0 + 1024;
  }
  const unsigned rdb2 = opaque(rdb0 + 2 * MID_BUF), rdb3 = opaque(rdb1 + 2 * MID_BUF);
  v4u bf[2][2][2];
  f32x4 acc[9][2] = {};
  constexpr int LA = 6, R = LA + 1;
  v4u af[R];
  const unsigned ra0 = lds0 + vl, ra1 = opaque(ra0 + 2 * MID_BUF);
  auto aaddr = [&](int i) -> unsigned {
    int t = i / 18, f = i % 18, b = t % NB;
    return ((b & 2) ? ra1 : ra0) + (b & 1) * MID_BUF + f * 1024;
  };
  auto sync = [&](int t) {
    rawbar();
    const int b = t % NB;
#pragma unroll
    for (int i = 0; i < 2; ++i) {
      bf[t & 1][i][0] = lds128(((b & 2) ? rdb2 : rdb0) + (b & 1) * MID_BUF + i * 2048);
      bf[t & 1][i][1] = lds128(((b & 2) ? rdb3 : rdb1) + (b & 1) * MID_BUF + i * 2048);
    }
  };
  sync(0);
#pragma unroll
  for (int i = 0; i < LA; ++i) af[i % R] = lds128(aaddr(i));
  static_for<0, NS>([&](auto tc) {
    constexpr int t = decltype(tc)::value;
#pragma unroll
    for (int f = 0; f < 18; ++f) {
      const int i = t * 18 + f;
      if (f == 18 - LA && t + 1 < NS) sync(t + 1);
      if (i + LA < NS * 18) af[(i + LA) % R] = lds128(aaddr(i + LA));
      SB();
      acc[f % 9][0] = mfma16(af[i % R], bf[t & 1][0][f / 9], acc[f % 9][0]);
      acc[f % 9][1] = mfma16(af[i % R], bf[t & 1][1][f / 9], acc[f % 9][1]);
      SB();
    }
  });
  const rsrc_t rs = mk_rsrc(part + (size_t)kq * SZ_X);
  constexpr float sc = SEQ ? 1.f : NLN2;
#pragma unroll
  for (int i = 0; i < 2; ++i) {
    const unsigned dst = ((8 * rt + 2 * w + i) * 72 + 18 * cb4) * 16;
#pragma unroll
    for (int P = 0; P < 4; ++P) {
      const f32x4 a = acc[2 * P][i] * sc, b = acc[2 * P + 1][i] * sc;
      v4u o = {pack2(a[0], a[1]), pack2(a[2], a[3]), pack2(b[0], b[1]), pack2(b[2], b[3])};
      st16(rs, dst + P * 64 + lane, o);
    }
    const f32x4 c = acc[8][i] * sc;
    v2u o2 = {pack2(c[0], c[1]), pack2(c[2], c[3])};
    st8(rs, (dst + 256) * 2 + ((q >> 1) * 16 + r) * 2 + (q & 1), o2);
  }
}

constexpr int GLU_PD = 4;
DEVI constexpr int glu_nf(int s) { return s < 9 ? 20 : 16; }
DEVI constexpr int glu_src(int s) { return s < 9 ? s * 20480 : 184320 + (s - 9) * 16384; }
DEVI void glu_issue_w(const char* wb, int s, unsigned lds0, int w, unsigned vl, int nw) {
  const int nf = glu_nf(s);
  for (int n = w; n < nf; n += nw) glds16<true>(wb + glu_src(s) + n * 1024, vl, lds0 + (s % GLU_NB) * GLU_BUF + n * 1024);
}
DEVI void glu_prefetch(const char* wgu, int cb, unsigned lds0, int lw, int lane) {
  const char* wb = wgu + (size_t)cb * (324 * 1024);
#pragma unroll
  for (int s = 0; s < GLU_PD; ++s)
#pragma unroll
    for (int i = 0; i < 5; ++i) glds16<true>(wb + glu_src(s) + (lw + 4 * i) * 1024, lane * 16, lds0 + s * GLU_BUF + (lw + 4 * i) * 1024);
}
DEVI unsigned glu_act2(float g0, float g1, float u0, float u1) {
  const float r0 = __builtin_amdgcn_rcpf(1.f + __builtin_amdgcn_exp2f(g0));
  const float r1 = __builtin_amdgcn_rcpf(1.f + __builtin_amdgcn_exp2f(g1));
  return pack2(u0 * r0, u1 * r1);
}
DEVI float add1(float a) { float r; asm("v_add_f32 %0, 1.0, %1" : "=v"(r) : "v"(a)); return r; }
DEVI float mul1(float a, float b) { float r; asm("v_mul_f32 %0, %1, %2" : "=v"(r) : "v"(a), "v"(b)); return r; }
template <class Pub>
DEVI void glu_body(const char* xb, const char* wgu, char* hb, unsigned lds0, int lane, int w, int panel, int cb, Pub&& publish0,
                   u64& early, unsigned early_off, const char* early_base) {
  const int r = lane & 15, q = lane >> 4;
  const unsigned vl = lane * 16;
  const char* wb = wgu + (size_t)cb * (324 * 1024) + w * 1024;
  const char* xb0 = xb + (size_t)(16 * panel + 2 * w) * (18 * 1024);
  const char* xb1 = xb0 + 18 * 1024;
  v4u xk[4][2];
  auto issue_x = [&](auto kc) {
    constexpr int kk = decltype(kc)::value, k32 = kk % 18;
    gload4<(k32 & 3) * 1024>(xk[kk & 3][0], vl, xb0 + (k32 >> 2) * 4096);
    gload4<(k32 & 3) * 1024>(xk[kk & 3][1], vl, xb1 + (k32 >> 2) * 4096);
  };
  auto issue_w = [&](int s, int i) {
    if (i < 2 || w < 4) glds16<true>(wb + glu_src(s) + i * 8192, vl, lds0 + (s % GLU_NB) * GLU_BUF + w * 1024 + i * 8192);
  };
  const unsigned ra0 = lds0 + vl, ra1 = opaque(ra0 + 3 * GLU_BUF);
  auto waddr = [&](int s, int f) -> unsigned {
    const int b = s % GLU_NB;
    return (b >= 3 ? ra1 : ra0) + (b % 3) * GLU_BUF + f * 1024;
  };
  f32x4 acc0[10][2], acc1[8][2];
#pragma unroll
  for (int j = 0; j < 10; ++j) { acc0[j][0] = f32x4{}; acc0[j][1] = f32x4{}; }
#pragma unroll
  for (int j = 0; j < 8; ++j) { acc1[j][0] = f32x4{}; acc1[j][1] = f32x4{}; }
  constexpr int LA = 4, R = LA + 1;
  v4u wf[R];
  const rsrc_t rs = mk_rsrc(hb);
  const unsigned hrow = (16 * panel + 2 * w) * (144 * 64) + (cb >> 3) * (36 * 64) + r;
  const unsigned c0 = (cb & 7) * 10 + q, c1 = 80 + (cb & 7) * 8 + q, cs = (cb & 7) * 10 + 8 + (q >> 1);
  auto haddr = [&](unsigned c, int n) -> unsigned { return hrow + n * (144 * 64) + (c >> 2) * 64 + (c & 3) * 16; };
  v4u ho;
  f32x2 ta[2], tb[2];
  auto epi_op = [&](auto ec, auto kc) {
    constexpr int e = decltype(ec)::value, k = decltype(kc)::value, z = e & 1;
    constexpr int t = e < 16 ? 2 * (e >> 3) + ((e & 3) >> 1) : 4;
    constexpr int n = e < 16 ? (e >> 2) & 1 : (e >> 1) & 1;
    constexpr int o = (e & 1) * 2, i = e < 16 ? e & 3 : e & 1;
    if constexpr (k == 0) ta[z][0] = __builtin_amdgcn_exp2f(acc0[2 * t][n][o]);
    if constexpr (k == 1) ta[z][1] = __builtin_amdgcn_exp2f(acc0[2 * t][n][o + 1]);
    if constexpr (k == 2) { tb[z][0] = add1(ta[z][0]); }
    if constexpr (k == 3) { tb[z][1] = add1(ta[z][1]); tb[z][0] = __builtin_amdgcn_rcpf(tb[z][0]); }
    if constexpr (k == 4) tb[z][1] = __builtin_amdgcn_rcpf(tb[z][1]);
    if constexpr (k == 5) tb[z][0] = mul1(tb[z][0], acc0[2 * t + 1][n][o]);
    if constexpr (k == 6) tb[z][1] = mul1(tb[z][1], acc0[2 * t + 1][n][o + 1]);
    if constexpr (k == 7) {
      ho[i] = pack2(tb[z][0], tb[z][1]);
      if constexpr (e < 16 && i == 3) st16(rs, haddr(c0 + 4 * (e >> 3), n), ho);
      if constexpr (e >= 16 && i == 1) st8(rs, haddr(cs, n) * 2 + (q & 1), v2u{ho[0], ho[1]});
    }
  };
  auto epi_step = [&](auto pc) {
    constexpr int p = decltype(pc)::value;
    static_for<0, 20>([&](auto ec) {
      constexpr int e = decltype(ec)::value, k = p - 1 - (9 * e) / 2;
      if constexpr (k >= 0 && k < 8) epi_op(ec, std::integral_constant<int, (k >= 0 && k < 8 ? k : 0)>{});
    });
  };
  issue_x(std::integral_constant<int, 0>{}); issue_x(std::integral_constant<int, 1>{}); issue_x(std::integral_constant<int, 2>{});
#pragma unroll
  for (int i = 0; i < LA; ++i) wf[i % R] = lds128(waddr(0, i));
  static_for<0, 18>([&](auto sc) {
    constexpr int s = decltype(sc)::value;
    constexpr int NF = glu_nf(s), HF = NF / 2;
    constexpr int base = s < 9 ? s * 20 : 180 + (s - 9) * 16;
    static_for<0, NF>([&](auto fc) {
      constexpr int f = decltype(fc)::value, i = base + f, kh = f / HF, j = f % HF, kk = 2 * s + kh;
      if constexpr (j == 0) {
        constexpr int nx = (kk + 2 < 36 ? 4 : (kk + 1 < 36 ? 2 : 0));
        constexpr int s1 = (kk - 2) / 2, s2 = (kk - 1) / 2;
        constexpr int nw = (kk >= 2 && ((kk - 2) & 1) == 0 && s1 + GLU_PD < 18 ? 2 : 0) + (kk >= 1 && ((kk - 1) & 1) == 0 && s2 + GLU_PD < 18 ? 2 : 0);
        wait_vm<nx + nw>();
        pin(xk[kk & 3][0]); pin(xk[kk & 3][1]);
        if constexpr (kk + 3 < 36) issue_x(std::integral_constant<int, (kk + 3 < 36 ? kk + 3 : 0)>{});
      }
      if constexpr (kh == 0 && s + GLU_PD < 18 && (j == 2 || j == 4 || j == 6)) {
        if constexpr (j < 6 || s + GLU_PD < 9) issue_w(s + GLU_PD, (j - 2) / 2);
      }
      if constexpr (f == NF - LA && s + 1 < 18) {
        rawbar();
        if constexpr (s == 16) publish0();
      }
      if constexpr (f + LA < NF) wf[(i + LA) % R] = lds128(waddr(s, f + LA));
      else if constexpr (s + 1 < 18) wf[(i + LA) % R] = lds128(waddr(s + 1, f + LA - NF));
      SB();
      if constexpr (s < 9) {
        acc0[j][0] = mfma16(wf[i % R], xk[kk & 3][0], acc0[j][0]);
        acc0[j][1] = mfma16(wf[i % R], xk[kk & 3][1], acc0[j][1]);
      } else {
        acc1[j][0] = mfma16(wf[i % R], xk[kk & 3][0], acc1[j][0]);
        acc1[j][1] = mfma16(wf[i % R], xk[kk & 3][1], acc1[j][1]);
        constexpr int p1 = (s - 9) * 16 + f;
        epi_step(std::integral_constant<int, p1>{});
      }
      SB();
    });
  });
  v4u fo[2][2];
#pragma unroll
  for (int P = 0; P < 2; ++P)
#pragma unroll
    for (int n = 0; n < 2; ++n)
#pragma unroll
      for (int i = 0; i < 4; ++i) {
        const int t = 2 * P + (i >> 1), oo = (i & 1) * 2;
        fo[P][n][i] = glu_act2(acc1[2 * t][n][oo], acc1[2 * t][n][oo + 1], acc1[2 * t + 1][n][oo], acc1[2 * t + 1][n][oo + 1]);
      }
  pin(fo[0][0]); pin(fo[0][1]); pin(fo[1][0]); pin(fo[1][1]);
  asm volatile("global_load_dwordx2 %0, %1, %2 sc1" : "=v"(early) : "v"(early_off), "s"(early_base) : "memory");
#pragma unroll
  for (int P = 0; P < 2; ++P)
#pragma unroll
    for (int n = 0; n < 2; ++n) st16(rs, haddr(c1 + 4 * P, n), fo[P][n]);
}

__global__ __launch_bounds__(512, 2) void k_all(const u16* __restrict__ x0, const char* __restrict__ Cr, const char* __restrict__ Ci,
                                                const u16* __restrict__ Wr, const u16* __restrict__ Wi, const u16* __restrict__ Wg,
                                                const u16* __restrict__ Wu, const u16* __restrict__ Wd, char* ws, char* out) {
  extern __shared__ __attribute__((aligned(16))) char smem[];
  const int lane = threadIdx.x & 63;
  const int w = __builtin_amdgcn_readfirstlane(threadIdx.x >> 6);
  const int lw = w & 3;
  const int g8 = blockIdx.x & 7, idx = blockIdx.x >> 3;
  const int kq = g8 >> 1, rt = 8 * (g8 & 1) + (idx >> 2), cb4 = idx & 3, j16 = kq * 4 + cb4;
  const int panel = (g8 & 1) * 4 + (idx >> 3), cb = (g8 >> 1) * 8 + (idx & 7);
  const unsigned lds0 = (unsigned)(uintptr_t)(LAS char*)smem;
  const unsigned tok0 = 128 * rt + 8 * j16;
  unsigned pfi = 0;
  if (w == 3) {
    u64 pc; unsigned len;
    asm volatile("s_getpc_b64 %0\n.Lk_all_pc:\n\ts_mov_b32 %1, .Lk_all_end-.Lk_all_pc" : "=s"(pc), "=s"(len));
    unsigned off = (idx + 32 * lane) * 128;
    off = off < len ? off : 0;
    asm volatile("s_nop 4\n\tglobal_load_dword %0, %1, %2" : "+v"(pfi) : "v"(off), "s"(pc) : "memory");
  }
  if (w == 3) {
    const unsigned nb = (unsigned)((FG_W + 16) * 256 * FST);
#pragma unroll
    for (int i = 0; i < 2; ++i) {
      unsigned off = (idx + 32 * (lane + 64 * i)) * 128;
      off = off < nb ? off : 0;
      asm volatile("global_load_dword %0, %1, %2 sc1" : "+v"(pfi) : "v"(off), "s"(ws) : "memory");
    }
  }
  u64 tag = 0;
  if (w == 4) tag = __hip_atomic_load((gu64*)(ws + OFF_FLAG), __ATOMIC_RELAXED, __HIP_MEMORY_SCOPE_AGENT) + 1;
  v4u xres[3] = {};
  u64 early = 0;
  Retile rtl;
  const int bid = blockIdx.x;
  const unsigned rstage = lds0 + LDS_WDT + lw * 9216;
  if (w < 3) {
    const unsigned tok = tok0 + (lane & 7), seg = w * 8 + (lane >> 3);
#pragma unroll
    for (int i = 0; i < 3; ++i) xres[i] = ((const v4u*)x0)[tok * 72 + seg + 24 * i];
    comb_phase<0>(0, tok0, xres, nullptr, ws + OFF_XN, lds0, w, lane);
    wait_vm<0>();
  } else {
    if (w >= 4) hid_load_w(Wr, Wi, j16, lds0, lw, lane);
    else rtl.issue(bid < 128 ? bid + 1024 : -1, Wg, Wu, Wd, lds0 + LDS_WDT + 4 * 9216, lane);
    rawbar();
  }
  rawbar();
  if (w == 4) publish(ws, FG_A + rt, j16, tag, lane);
  unsigned pfd = 0;
  if (w >= 4) {
    rtl.issue(bid + 256 * lw, Wg, Wu, Wd, rstage, lane);
    const char* cbp = ((kq >> 1) ? Ci : Cr) + ((size_t)(128 * rt + 32 * cb4 + 8 * lw) * S + (kq & 1) * 1024) * 2;
    const unsigned po = (lane >> 3) * 4096 + (lane & 7) * 128;
    asm volatile("global_load_dword %0, %1, %2" : "+v"(pfd) : "v"(po), "s"(cbp) : "memory");
  }
  if (w == 3) {
    asm volatile("s_waitcnt vmcnt(0)" : "+v"(pfi)::"memory");
    rtl.finish(ws, lds0 + LDS_WDT + 4 * 9216, lane);
  }
#pragma unroll 1
  for (int l = 0; l < 2; ++l) {
    const int fb = l * FG_LAYER;
    char* ab = ws + OFF_AB + l * SZ_AB;
    char* hb = ws + OFF_H + l * SZ_H;
    char* p0 = ws + OFF_P + (2 * l) * SZ_P;
    char* p1 = p0 + SZ_P;
    char* xna = ws + OFF_XN + (2 * l) * SZ_X;
    char* xnb = xna + SZ_X;
    if (w == 4) wait_flags(ws, fb + FG_A + rt, 16, tag, lane);
    if (w >= 4) { if (l == 0) wait_vm<10>(); else wait_vm<0>(); }
    rawbar();
    if (w < 4) hid_compute(opq(xna), opq(ab), opqs(rt), opqs(j16), (unsigned)opqs(lds0), opqs(w), opaque(lane));
    else if (l == 0) { wait_vm<1>(); rtl.finish(opq(ws), rstage, opaque(lane)); }
    asm volatile("s_waitcnt vmcnt(0)" : "+v"(pfd), "+v"(pfi)::"memory");
    bar();
    if (w >= 4) {
      if (w == 4) publish(ws, fb + FG_B + (j16 >> 3) * 8 + (rt >> 3) * 4 + ((j16 >> 1) & 3), (rt & 7) * 2 + (j16 & 1), tag, lane);
      MidLoader<true> ld;
      ld.init(opq(Cr), opq(Ci), opq(ab), opqs(kq), opqs(rt), opqs(cb4), (unsigned)opqs(lds0), opqs(lw), opaque(lane));
      ld.prefetch();
      if (w == 4) wait_flags(ws, fb + FG_B + kq * 4 + cb4, 16, tag, lane);
      rawbar();
      ld.run([&](int) {});
    } else {
      rawbar();
      mid_compute<true>(opq(p0), opqs(kq), opqs(rt), opqs(cb4), (unsigned)opqs(lds0), opqs(w), opaque(lane));
      wait_vm<0>();
    }
    bar();
    if (w == 4) publish(ws, fb + FG_C + rt, j16, tag, lane);
    if (w >= 4 && l == 1) glu_prefetch(opq(ws) + OFF_WGU, opqs(cb), (unsigned)opqs(lds0), opqs(lw), opaque(lane));
    if (w >= 5 && l == 0) rtl.issue(rt < 12 ? 1152 + cb4 * 144 + 36 * kq + 3 * rt + (lw - 1) : -1, opq(Wg), opq(Wu), opq(Wd), rstage, opaque(lane));
    if (w == 4) wait_flags(ws, fb + FG_C + rt, 16, tag, lane);
    rawbar();
    if (w < 3) { comb_phase<4>(1, opqs(tok0), xres, opq(p0), opq(xnb), (unsigned)opqs(lds0), opqs(w), opaque(lane)); wait_vm<0>(); }
    else {
      if (w >= 5 && l == 0) { wait_vm<0>(); rtl.finish(opq(ws), rstage, opaque(lane)); }
      bar();
      if (w >= 4 && l == 0) { wait_vm<0>(); glu_prefetch(opq(ws) + OFF_WGU, opqs(cb), (unsigned)opqs(lds0), opqs(lw), opaque(lane)); }
    }
    rawbar();
    if (w == 4) {
      publish(ws, fb + FG_D + (rt >> 1), (rt & 1) * 16 + j16, tag, lane);
      if (l == 0) publish(ws, FG_W + j16, rt, tag, lane);
    }
    if (w == 4) wait_flags(ws, fb + FG_D + panel, 32, tag, lane);
    if (w >= 4) wait_vm<0>();
    rawbar();
    glu_body(opq(xnb), opq(ws) + OFF_WGU, opq(hb), (unsigned)opqs(lds0), opaque(lane), opqs(w), opqs(panel), opqs(cb),
             [&]() { if (w == 4) publish(ws, fb + FG_E + panel * 4 + (cb >> 3), cb & 7, tag, lane); },
             early, (unsigned)(((fb + FG_E + (rt >> 1) * 4 + kq) * 256 + (lane & 7) * 8) * FST), ws + OFF_FLAG);
    bar();
    if (w >= 4) {
      MidLoader<false> ld;
      ld.init(opq(hb), nullptr, opq(ws) + OFF_WDT, opqs(kq), opqs(rt), opqs(cb4), (unsigned)opqs(lds0), opqs(lw), opaque(lane));
      if (w == 4) {
        if (l == 0) wait_flags(ws, FG_W + j16, 16, tag, lane);
        asm volatile("s_waitcnt vmcnt(4)" : "+v"(early)::"memory");
        if (!__all(early == tag)) wait_flags(ws, fb + FG_E + (rt >> 1) * 4 + kq, 8, tag, lane);
      }
      if (l == 0) rawbar();
      ld.prefetch();
      rawbar();
      ld.run([&](int t) {
        if (w == 4) {
          if (t == 0) publish(ws, fb + FG_E + panel * 4 + (cb >> 3), 8 + (cb & 7), tag, lane);
          if (t == 7) wait_flags(ws, fb + FG_E + (rt >> 1) * 4 + kq, 8, tag, lane, 8);
        }
      });
      asm volatile("" : "+v"(early));
    } else {
      if (l == 0) rawbar();
      rawbar();
      asm volatile("s_waitcnt vmcnt(0)" : "+v"(early)::"memory");
      mid_compute<false>(opq(p1), opqs(kq), opqs(rt), opqs(cb4), (unsigned)opqs(lds0), opqs(w), opaque(lane));
      wait_vm<0>();
    }
    bar();
    if (w == 4) publish(ws, fb + FG_F + rt, j16, tag, lane);
    if (w >= 4 && l == 0) hid_load_w(opq(Wr), opq(Wi), opqs(j16), (unsigned)opqs(lds0), opqs(lw), opaque(lane));
    if (w == 4) wait_flags(ws, fb + FG_F + rt, 16, tag, lane);
    rawbar();
    if (w < 3) { comb_phase<4>(l == 0 ? 0 : 2, opqs(tok0), xres, opq(p1), opq(l == 0 ? xnb + SZ_X : out), (unsigned)opqs(lds0), opqs(w), opaque(lane)); wait_vm<0>(); }
    else rawbar();
    rawbar();
    if (w == 4 && l == 0) publish(ws, FG_LAYER + FG_A + rt, j16, tag, lane);
  }
  if (blockIdx.x == 0 && w == 4 && lane == 0)
    __hip_atomic_store((gu64*)(ws + OFF_FLAG), tag, __ATOMIC_RELAXED, __HIP_MEMORY_SCOPE_AGENT);
  asm volatile(".Lk_all_end:");
}

extern "C" void kernel_launch(void* const* d_in, const int* in_sizes, int n_in, void* d_out, int out_size, void* d_ws,
                              size_t ws_size, hipStream_t stream) {
  (void)hipFuncSetAttribute((const void*)k_all, hipFuncAttributeMaxDynamicSharedMemorySize, LDS_TOTAL);
  k_all<<<256, 512, LDS_TOTAL, stream>>>((const u16*)d_in[0], (const char*)d_in[1], (const char*)d_in[2], (const u16*)d_in[3],
                                         (const u16*)d_in[4], (const u16*)d_in[6], (const u16*)d_in[7], (const u16*)d_in[8],
                                         (char*)d_ws, (char*)d_out);
}
```
